```python
import torch

def _softmax(x, axis=-1):
    return torch.softmax(x, dim=axis)
def _gelu(x):
    return torch.nn.functional.gelu(x, approximate="tanh")
def _silu(x):
    return torch.nn.functional.silu(x)
def _sigmoid(x):
    return torch.sigmoid(x)
def _layernorm(x, axis=-1, eps=1e-5):
    m = x.mean(dim=axis, keepdim=True)
    v = x.var(dim=axis, keepdim=True, unbiased=False)
    return (x - m) / torch.sqrt(v + eps)
def _scatter_add(tgt, idx, src, axis=0):
    return tgt.clone().index_add_(axis, idx, src)
def _segment_sum(src, seg, n):
    out = torch.zeros((n,) + src.shape[1:], dtype=src.dtype)
    return out.index_add_(0, seg, src)
def _pad(x, pw):
    flat = tuple(v for pair in reversed(pw) for v in pair)
    return torch.nn.functional.pad(x, flat)

kernel_name = 'recurrent_scan__linear_attn_block__glu_gated_swiglu__prompt'

def setup_inputs():

    _w = (lambda a: (lambda t: t.to(torch.bfloat16) if t.is_floating_point() else t)(torch.as_tensor(a)))
    def _A_setup():
        B, S, D = 1, 2048, 448
        return {
            "x": _w((lambda *s: torch.randn(*s, dtype=torch.bfloat16) * 0.05)(B, S, D)),
            "wqkv": _w((lambda *s: torch.randn(*s, dtype=torch.bfloat16) * 0.8)(D, 3 * D)),
            "wo": _w((lambda *s: torch.randn(*s, dtype=torch.bfloat16) * 0.8)(D, D)),
        }
    _A_in = _A_setup()

    def _B_setup():
        B, S, D, F = 1, 2048, 448, 1792
        return {
            "x": _w((lambda *s: torch.randn(*s, dtype=torch.bfloat16) * 0.05)(B, S, D)),
            "w1": _w((lambda *s: torch.randn(*s, dtype=torch.bfloat16) * 0.2)(D, F) * (2.0 / D) ** 0.5),
            "w3": _w((lambda *s: torch.randn(*s, dtype=torch.bfloat16) * 0.2)(D, F) * (2.0 / D) ** 0.5),
            "w2": _w((lambda *s: torch.randn(*s, dtype=torch.bfloat16) * 0.2)(F, D) * (2.0 / F) ** 0.5),
        }
    _B_in = _B_setup()

    return {
        "A_x": _A_in["x"],
        "A_wqkv": _A_in["wqkv"],
        "A_wo": _A_in["wo"],
        "B_x": _B_in["x"],
        "B_w1": _B_in["w1"],
        "B_w3": _B_in["w3"],
        "B_w2": _B_in["w2"],
    }

def reference(A_x, A_wqkv, A_wo, B_x, B_w1, B_w3, B_w2):
    def _A_ref(x, wqkv, wo):
        B, S, D = x.shape
        H = 16; Dh = D // H
        qkv = (lambda x, *d: x.permute(*d))((x @ wqkv).reshape(B, S, 3, H, Dh), 2, 0, 3, 1, 4)
        q, k, v = qkv[0], qkv[1], qkv[2]
        phi = lambda t: torch.where(t > 0, t + 1, torch.exp(t)) + 1e-6
        pq, pk = phi(q), phi(k)
        kv = torch.einsum("bhsd,bhse->bhde", pk, v)
        z = torch.einsum("bhsd->bhd", pk)
        num = torch.einsum("bhqd,bhde->bhqe", pq, kv)
        den = torch.einsum("bhqd,bhd->bhq", pq, z)[..., None] + 1e-6
        o = num / den
        return (lambda x, *d: x.permute(*d))(o, 0, 2, 1, 3).reshape(B, S, D) @ wo

    def _B_ref(x, w1, w3, w2):
        gate = torch.einsum("bsd,df->bsf", x, w1)
        up = torch.einsum("bsd,df->bsf", x, w3)
        h = _silu(gate) * up
        out = torch.einsum("bsf,fd->bsd", h, w2)
        return out

    x = A_x
    xn = _layernorm(x, axis=-1)
    s = _A_ref(xn, A_wqkv, A_wo)
    g = _silu(_B_ref(xn, B_w1, B_w3, B_w2))
    return x + s * g
```

```cpp
#include <hip/hip_runtime.h>
#include <hip/hip_bf16.h>
#include <stdint.h>
#include <type_traits>

typedef __attribute__((ext_vector_type(8))) __bf16 bf16x8;
typedef __attribute__((ext_vector_type(4))) __bf16 bf16x4;
typedef __attribute__((ext_vector_type(4))) short s16x4;
typedef __attribute__((ext_vector_type(8))) short s16x8;
typedef __attribute__((ext_vector_type(4))) float f32x4;
typedef __attribute__((ext_vector_type(4))) unsigned u32x4;
typedef __attribute__((ext_vector_type(2))) unsigned u32x2;
typedef __attribute__((address_space(3))) s16x4* lds_s16x4_ptr;
typedef __attribute__((address_space(3))) void* lds_void_ptr;
typedef const __attribute__((address_space(1))) void* gbl_cvoid_ptr;

constexpr int SEQ = 2048, DM = 448, FF = 1792, NH = 16, DH = 28;

constexpr size_t WS_H    = 0;
constexpr size_t WS_PQ   = 7340032;
constexpr size_t WS_KVP  = WS_PQ + 2097152;
constexpr size_t WS_KVR  = WS_KVP + 1048576;
constexpr size_t WS_FLAG = WS_KVR + 32768;

template <int I, int N, class Fn>
__device__ __forceinline__ void static_for(Fn&& f) {
  if constexpr (I < N) { f(std::integral_constant<int, I>{}); static_for<I + 1, N>(f); }
}
template <int N> __device__ __forceinline__ void wait_vm() { asm volatile("s_waitcnt vmcnt(%0)" ::"n"(N) : "memory"); }
__device__ __forceinline__ void bar() { asm volatile("s_waitcnt lgkmcnt(0)" ::: "memory"); __builtin_amdgcn_s_barrier(); }

__device__ __forceinline__ float bfr(float f) { return (float)(__bf16)f; }
__device__ __forceinline__ float bf_lo(unsigned u) { return __uint_as_float(u << 16); }
__device__ __forceinline__ float bf_hi(unsigned u) { return __uint_as_float(u & 0xffff0000u); }
__device__ __forceinline__ unsigned pack2(float a, float b) {
  typedef __attribute__((ext_vector_type(2))) __bf16 bf16x2;
  bf16x2 v; v[0] = (__bf16)a; v[1] = (__bf16)b;
  return __builtin_bit_cast(unsigned, v);
}
__device__ __forceinline__ void glds16(const void* g, void* l) {
  __builtin_amdgcn_global_load_lds((gbl_cvoid_ptr)g, (lds_void_ptr)l, 16, 0, 0);
}
__device__ __forceinline__ bf16x8 tr_frag(const char* lds_a, const char* lds_b) {
  s16x4 lo = __builtin_amdgcn_ds_read_tr16_b64_v4i16((lds_s16x4_ptr)lds_a);
  s16x4 hi = __builtin_amdgcn_ds_read_tr16_b64_v4i16((lds_s16x4_ptr)lds_b);
  s16x8 v = __builtin_shufflevector(lo, hi, 0, 1, 2, 3, 4, 5, 6, 7);
  return __builtin_bit_cast(bf16x8, v);
}
__device__ __forceinline__ f32x4 mfma16(bf16x8 a, bf16x8 b, f32x4 c) {
  return __builtin_amdgcn_mfma_f32_16x16x32_bf16(a, b, c, 0, 0, 0);
}
__device__ __forceinline__ float sigmoidf_(float v) { return 1.0f / (1.0f + __expf(-v)); }
__device__ __forceinline__ float silu_bf(float v) {
  return bfr(v * sigmoidf_(v));
}
__device__ __forceinline__ float phi_bf(float acc) {
  float t = bfr(acc);
  float p = t > 0.0f ? bfr(t + 1.0f) : bfr(__expf(t));
  return bfr(p + 1e-6f);
}

constexpr int K1_SLOT = 24576;
constexpr int K1_XN_BYTES = 114688;
__host__ __device__ constexpr int k1_slot_off(int s) { return s < 2 ? K1_XN_BYTES + K1_SLOT * s : K1_SLOT * (s - 2); }
__host__ __device__ constexpr int k1_seg_kind(int type, int seg) {
  return type == 0 ? (seg == 0 ? 0 : 1) : (seg < 2 ? 1 : 2);
}
__host__ __device__ constexpr int k1_nstage(int type) { return type == 0 ? 14 : 21; }
__host__ __device__ constexpr int k1_cnt(int type, int t) {
  return t >= k1_nstage(type) ? 0 : (k1_seg_kind(type, t / 7) == 0 ? 3 : (k1_seg_kind(type, t / 7) == 1 ? 2 : 1));
}
__host__ __device__ constexpr int k1_wait(int type, int t) {
  return k1_cnt(type, t + 1) + k1_cnt(type, t + 2) + k1_cnt(type, t + 3) + k1_cnt(type, t + 4);
}

template <int TYPE>
__device__ __forceinline__ void k1_body(char* smem, const __hip_bfloat16* __restrict__ x,
                                        const __hip_bfloat16* __restrict__ wqkv,
                                        const __hip_bfloat16* __restrict__ w1,
                                        const __hip_bfloat16* __restrict__ w3, char* ws, int mtile,
                                        int role) {
  const int tid = threadIdx.x, w = __builtin_amdgcn_readfirstlane(tid >> 6), l = tid & 63;
  const int rg = w & 3, cg = w >> 2;
  const int g = l >> 4, q = (l & 15) >> 2, p = l & 3;
  constexpr int NSEG = TYPE == 0 ? 2 : 3;

  const char* src[NSEG][3];
  int unit0 = TYPE == 0 ? role : 8 + 2 * (role - 8);
  static_for<0, NSEG>([&](auto sc) {
    constexpr int seg = sc.value;
    constexpr int kind = k1_seg_kind(TYPE, seg);
    if constexpr (kind == 1) {
      int n0 = (TYPE == 0 ? unit0 : unit0 + seg) * 64;
      int f = (l >> 4) | ((w & 1) << 2);
      int ch = (l & 15) ^ (f << 1);
      const __hip_bfloat16* base = ch < 8 ? w1 : w3;
      for (int b = 0; b < 2; ++b) {
        int row = 8 * w + 4 * b + (l >> 4);
        src[seg][b] = (const char*)(base + (size_t)row * FF + n0 + 8 * (ch & 7));
      }
    } else if constexpr (kind == 2) {
      int n0 = 1536 + 32 * (role - 8);
      int rr = l >> 3;
      int key = ((rr >> 1) & 1) | ((w & 1) << 1);
      int ch = (l & 7) ^ (key << 1);
      const __hip_bfloat16* base = ch < 4 ? w1 : w3;
      int row = 8 * w + rr;
      src[seg][0] = (const char*)(base + (size_t)row * FF + n0 + 8 * (ch & 3));
    } else {
      for (int b = 0; b < 3; ++b) {
        int cl = 64 * (3 * w + b) + l;
        int row = cl / 24, chp = cl % 24;
        int blk = chp >> 3;
        int key = ((row >> 1) & 1) | (((row >> 3) & 1) << 1);
        int cc = (chp & 7) ^ (key << 1);
        int head = 2 * role + (cc >> 2);
        int col = blk * DM + DH * head + 8 * (cc & 3);
        if (blk == 2 && head == 15) col -= 4;
        src[seg][b] = (const char*)(wqkv + (size_t)row * (3 * DM) + col);
      }
    }
  });

  auto issue = [&](auto tc) {
    constexpr int t = tc.value;
    if constexpr (t < k1_nstage(TYPE)) {
      constexpr int seg = t / 7, s = t % 7, kind = k1_seg_kind(TYPE, seg);
      char* slot = smem + k1_slot_off(t % 6);
      if constexpr (kind == 1) {
        glds16(src[seg][0] + (size_t)s * 64 * FF * 2, slot + (8 * w) * 256);
        glds16(src[seg][1] + (size_t)s * 64 * FF * 2, slot + (8 * w + 4) * 256);
      } else if constexpr (kind == 2) {
        glds16(src[seg][0] + (size_t)s * 64 * FF * 2, slot + w * 1024);
      } else {
        glds16(src[seg][0] + (size_t)s * 64 * (3 * DM) * 2, slot + (3 * w) * 1024);
        glds16(src[seg][1] + (size_t)s * 64 * (3 * DM) * 2, slot + (3 * w + 1) * 1024);
        glds16(src[seg][2] + (size_t)s * 64 * (3 * DM) * 2, slot + (3 * w + 2) * 1024);
      }
    }
  };

  issue(std::integral_constant<int, 0>{});
  issue(std::integral_constant<int, 1>{});

  {
    const int row = l >> 2, sub = l & 3;
    const char* xr = (const char*)(x + (size_t)(mtile * 128 + 16 * w + row) * DM) + sub * 16;
    u32x4 raw[14];
#pragma unroll
    for (int i = 0; i < 14; ++i) raw[i] = *(const u32x4*)(xr + i * 64);
    float d[112];
    float s0 = 0.f, s1 = 0.f;
#pragma unroll
    for (int i = 0; i < 14; ++i)
#pragma unroll
      for (int j = 0; j < 4; ++j) {
        d[i * 8 + 2 * j] = bf_lo(raw[i][j]); d[i * 8 + 2 * j + 1] = bf_hi(raw[i][j]);
        s0 += d[i * 8 + 2 * j]; s1 += d[i * 8 + 2 * j + 1];
      }
    float sum = s0 + s1;
    sum += __shfl_xor(sum, 1); sum += __shfl_xor(sum, 2);
    const float mean = sum * (1.0f / DM);
    float v0 = 0.f, v1 = 0.f;
#pragma unroll
    for (int i = 0; i < 112; i += 2) {
      d[i] -= mean; d[i + 1] -= mean;
      v0 += d[i] * d[i]; v1 += d[i + 1] * d[i + 1];
    }
    float var = v0 + v1;
    var += __shfl_xor(var, 1); var += __shfl_xor(var, 2);
    var = bfr(var * (1.0f / DM));
    const float den = bfr(sqrtf(bfr(var + 1e-5f)));
    const float inv = 1.0f / den;
    const float c0 = (mean - bfr(mean)) * inv;
    char* dst = smem + w * 14336 + (((row ^ (2 * sub)) + 16 * sub) << 4);
#pragma unroll
    for (int i = 0; i < 14; ++i) {
      u32x4 o;
#pragma unroll
      for (int j = 0; j < 4; ++j)
        o[j] = pack2(d[i * 8 + 2 * j] * inv + c0, d[i * 8 + 2 * j + 1] * inv + c0);
      *(u32x4*)(dst + i * 1024) = o;
    }
  }
  bar();
  bf16x8 af[2][14];
  {
    const int r = l & 15;
    const char* s0 = smem + (2 * rg) * 14336 + (((r ^ (2 * g)) + 16 * g) << 4);
#pragma unroll
    for (int rt = 0; rt < 2; ++rt)
#pragma unroll
      for (int i = 0; i < 14; ++i) af[rt][i] = *(const bf16x8*)(s0 + rt * 14336 + i * 1024);
  }
  bar();
  issue(std::integral_constant<int, 2>{});
  issue(std::integral_constant<int, 3>{});
  issue(std::integral_constant<int, 4>{});

  const int rt16 = mtile * 8 + rg * 2;

  const int f_ffn = q | ((g & 1) << 2);
  const int key2 = ((q >> 1) & 1) | ((g & 1) << 1);

  static_for<0, NSEG>([&](auto sc) {
    constexpr int seg = sc.value;
    constexpr int kind = k1_seg_kind(TYPE, seg);
    if constexpr (kind == 1) {
      f32x4 acc[2][4];
#pragma unroll
      for (int a = 0; a < 2; ++a)
#pragma unroll
        for (int b = 0; b < 4; ++b) acc[a][b] = f32x4{0.f, 0.f, 0.f, 0.f};
      int off[4];
#pragma unroll
      for (int c = 0; c < 4; ++c) {
        int tile = (c >> 1) * 4 + 2 * cg + (c & 1);
        off[c] = (8 * g + q) * 256 + ((tile ^ f_ffn) << 5) + 8 * p;
      }
      static_for<0, 7>([&](auto ssc) {
        constexpr int s = ssc.value, t = seg * 7 + s;
        wait_vm<k1_wait(TYPE, t)>();
        bar();
        issue(std::integral_constant<int, t + 5>{});
        const char* slot = smem + k1_slot_off(t % 6);
#pragma unroll
        for (int ks = 0; ks < 2; ++ks)
#pragma unroll
          for (int c = 0; c < 4; ++c) {
            bf16x8 bfg = tr_frag(slot + ks * 8192 + off[c], slot + ks * 8192 + 1024 + off[c]);
            acc[0][c] = mfma16(bfg, af[0][2 * s + ks], acc[0][c]);
            acc[1][c] = mfma16(bfg, af[1][2 * s + ks], acc[1][c]);
          }
      });
      const int unit = TYPE == 0 ? unit0 : unit0 + seg;
      const int kstep = unit * 2 + cg;
#pragma unroll
      for (int rt = 0; rt < 2; ++rt) {
        float h[8];
#pragma unroll
        for (int c = 0; c < 2; ++c)
#pragma unroll
          for (int j = 0; j < 4; ++j) {
            float gt = bfr(acc[rt][c][j]), up = bfr(acc[rt][2 + c][j]);
            h[c * 4 + j] = silu_bf(gt) * up;
          }
        u32x4 o = {pack2(h[0], h[1]), pack2(h[2], h[3]), pack2(h[4], h[5]), pack2(h[6], h[7])};
        *(u32x4*)(ws + WS_H + ((size_t)((rt16 + rt) * 56 + kstep) * 64 + l) * 16) = o;
      }
    } else if constexpr (kind == 2) {
      f32x4 acc[2][4];
#pragma unroll
      for (int a = 0; a < 2; ++a)
#pragma unroll
        for (int b = 0; b < 4; ++b) acc[a][b] = f32x4{0.f, 0.f, 0.f, 0.f};
      int off[4];
#pragma unroll
      for (int c = 0; c < 4; ++c) off[c] = (8 * g + q) * 128 + ((c ^ key2) << 5) + 8 * p;
      static_for<0, 7>([&](auto ssc) {
        constexpr int s = ssc.value, t = seg * 7 + s;
        wait_vm<k1_wait(TYPE, t)>();
        bar();
        issue(std::integral_constant<int, t + 5>{});
        const char* slot = smem + k1_slot_off(t % 6);
        if (cg == 0) {
#pragma unroll
          for (int ks = 0; ks < 2; ++ks)
#pragma unroll
            for (int c = 0; c < 4; ++c) {
              bf16x8 bfg = tr_frag(slot + ks * 4096 + off[c], slot + ks * 4096 + 512 + off[c]);
              acc[0][c] = mfma16(bfg, af[0][2 * s + ks], acc[0][c]);
              acc[1][c] = mfma16(bfg, af[1][2 * s + ks], acc[1][c]);
            }
        }
      });
      if (cg == 0) {
        const int kstep = 48 + (role - 8);
#pragma unroll
        for (int rt = 0; rt < 2; ++rt) {
          float h[8];
#pragma unroll
          for (int c = 0; c < 2; ++c)
#pragma unroll
            for (int j = 0; j < 4; ++j) {
              float gt = bfr(acc[rt][c][j]), up = bfr(acc[rt][2 + c][j]);
              h[c * 4 + j] = silu_bf(gt) * up;
            }
          u32x4 o = {pack2(h[0], h[1]), pack2(h[2], h[3]), pack2(h[4], h[5]), pack2(h[6], h[7])};
          *(u32x4*)(ws + WS_H + ((size_t)((rt16 + rt) * 56 + kstep) * 64 + l) * 16) = o;
        }
      }
    } else {
      const int head = 2 * role + cg;
      f32x4 acc[2][6];
#pragma unroll
      for (int a = 0; a < 2; ++a)
#pragma unroll
        for (int b = 0; b < 6; ++b) acc[a][b] = f32x4{0.f, 0.f, 0.f, 0.f};
      int off[6];
#pragma unroll
      for (int c = 0; c < 6; ++c) {
        int blk = c >> 1, ct = c & 1;
        int pi = 4 * ct + p + ((blk == 2 && head == 15) ? 1 : 0);
        pi = pi > 7 ? 7 : pi;
        int cc = 4 * cg + (pi >> 1);
        off[c] = (8 * g + q) * 384 + blk * 128 + ((cc ^ (key2 << 1)) << 4) + 8 * (pi & 1);
      }
      static_for<0, 7>([&](auto ssc) {
        constexpr int s = ssc.value, t = seg * 7 + s;
        wait_vm<k1_wait(TYPE, t)>();
        bar();
        issue(std::integral_constant<int, t + 5>{});
        const char* slot = smem + k1_slot_off(t % 6);
#pragma unroll
        for (int ks = 0; ks < 2; ++ks)
#pragma unroll
          for (int c = 0; c < 6; ++c) {
            bf16x8 bfg = tr_frag(slot + ks * 12288 + off[c], slot + ks * 12288 + 1536 + off[c]);
            if (c < 2) {
              acc[0][c] = mfma16(bfg, af[0][2 * s + ks], acc[0][c]);
              acc[1][c] = mfma16(bfg, af[1][2 * s + ks], acc[1][c]);
            } else {
              acc[0][c] = mfma16(af[0][2 * s + ks], bfg, acc[0][c]);
              acc[1][c] = mfma16(af[1][2 * s + ks], bfg, acc[1][c]);
            }
          }
      });
#pragma unroll
      for (int rt = 0; rt < 2; ++rt) {
        float v[8];
#pragma unroll
        for (int j = 0; j < 4; ++j) {
          v[j] = phi_bf(acc[rt][0][j]);
          float t1 = phi_bf(acc[rt][1][j]);
          v[4 + j] = g == 3 ? 0.f : t1;
        }
        u32x4 o = {pack2(v[0], v[1]), pack2(v[2], v[3]), pack2(v[4], v[5]), pack2(v[6], v[7])};
        *(u32x4*)(ws + WS_PQ + ((size_t)((rt16 + rt) * 16 + head) * 64 + l) * 16) = o;
      }
      bf16x8 pk[2], vv[2];
#pragma unroll
      for (int ct = 0; ct < 2; ++ct) {
        u32x4 a = {pack2(phi_bf(acc[0][2 + ct][0]), phi_bf(acc[0][2 + ct][1])),
                   pack2(phi_bf(acc[0][2 + ct][2]), phi_bf(acc[0][2 + ct][3])),
                   pack2(phi_bf(acc[1][2 + ct][0]), phi_bf(acc[1][2 + ct][1])),
                   pack2(phi_bf(acc[1][2 + ct][2]), phi_bf(acc[1][2 + ct][3]))};
        pk[ct] = __builtin_bit_cast(bf16x8, a);
        u32x4 b = {pack2(acc[0][4 + ct][0], acc[0][4 + ct][1]), pack2(acc[0][4 + ct][2], acc[0][4 + ct][3]),
                   pack2(acc[1][4 + ct][0], acc[1][4 + ct][1]), pack2(acc[1][4 + ct][2], acc[1][4 + ct][3])};
        if (ct == 1 && (l & 15) == 12) b = u32x4{0x3f803f80u, 0x3f803f80u, 0x3f803f80u, 0x3f803f80u};
        vv[ct] = __builtin_bit_cast(bf16x8, b);
      }
      f32x4 kvd[4];
#pragma unroll
      for (int dt = 0; dt < 2; ++dt)
#pragma unroll
        for (int et = 0; et < 2; ++et) kvd[dt * 2 + et] = mfma16(pk[dt], vv[et], f32x4{0.f, 0.f, 0.f, 0.f});
      bar();
      {
        char* sc = smem + k1_slot_off(w >> 1) + 16384 + (w & 1) * 4096;
#pragma unroll
        for (int i = 0; i < 4; ++i) *(f32x4*)(sc + i * 1024 + l * 16) = kvd[i];
      }
      bar();
      {
        const int hh = tid >> 8, idx = tid & 255;
        f32x4 sum = f32x4{0.f, 0.f, 0.f, 0.f};
#pragma unroll
        for (int r = 0; r < 4; ++r) {
          int ww = hh * 4 + r;
          sum += *(const f32x4*)(smem + k1_slot_off(ww >> 1) + 16384 + (ww & 1) * 4096 + idx * 16);
        }
        *(f32x4*)(ws + WS_KVP + ((size_t)(mtile * 16 + 2 * role + hh) * 256 + idx) * 16) = sum;
      }
    }
  });
}

__global__ __launch_bounds__(512, 2) void k1_kernel(const __hip_bfloat16* __restrict__ x,
                                                    const __hip_bfloat16* __restrict__ wqkv,
                                                    const __hip_bfloat16* __restrict__ w1,
                                                    const __hip_bfloat16* __restrict__ w3, char* ws) {
  __shared__ __attribute__((aligned(16))) char smem[163840];
  const int b = blockIdx.x;
  const int role = 2 * (b & 7) + ((b >> 3) & 1), mtile = b >> 4;
  if (b == 0 && threadIdx.x == 0) *(unsigned*)(ws + WS_FLAG) = 0u;
  if (role < 8) k1_body<0>(smem, x, wqkv, w1, w3, ws, mtile, role);
  else k1_body<1>(smem, x, wqkv, w1, w3, ws, mtile, role);
}

constexpr int K2_SLOT = 32768;
constexpr int K2_NGEMM = 224;

__device__ __forceinline__ void k2_gemm(char* smem, const __hip_bfloat16* __restrict__ x,
                                        const __hip_bfloat16* __restrict__ wo,
                                        const __hip_bfloat16* __restrict__ w2, char* ws,
                                        __hip_bfloat16* __restrict__ out) {
  const int tid = threadIdx.x, w = __builtin_amdgcn_readfirstlane(tid >> 6), l = tid & 63;
  const int b = blockIdx.x;
  const int xcd = b & 7, idx = b >> 3;
  const int mt = 4 * xcd + idx / 7, nt = idx % 7;
  const int rt = w & 3, kg = w >> 2;
  const int g = l >> 4, q = (l & 15) >> 2, p = l & 3;
  const int n0 = nt * 64;

  const char* src_w2[2];
  const char* src_wo[2][4];
  {
    int chs = (l & 7) ^ (((l >> 4) & 3) << 1);
    for (int i = 0; i < 2; ++i) {
      int row = 8 * (2 * w + i) + (l >> 3);
      src_w2[i] = (const char*)(w2 + (size_t)row * DM + n0 + 8 * chs);
    }
    for (int st = 0; st < 2; ++st)
      for (int i = 0; i < 4; ++i) {
        int row = 224 * st + 8 * (4 * w + i) + (l >> 3);
        row = row > 447 ? 447 : row;
        src_wo[st][i] = (const char*)(wo + (size_t)row * DM + n0 + 8 * chs);
      }
  }
  const char* src_h = ws + WS_H + ((size_t)((mt * 4 + rt) * 56 + kg) * 64 + l) * 16;

  auto issue = [&](auto tc) {
    constexpr int t = tc.value;
    char* slot = smem + (t & 3) * K2_SLOT;
    if constexpr (t < 14) {
      glds16(src_w2[0] + (size_t)t * 128 * DM * 2, slot + (2 * w) * 1024);
      glds16(src_w2[1] + (size_t)t * 128 * DM * 2, slot + (2 * w + 1) * 1024);
      glds16(src_h + (size_t)(t * 4) * 1024, slot + 16384 + (rt * 4 + kg) * 1024);
      glds16(src_h + (size_t)(t * 4 + 2) * 1024, slot + 16384 + (rt * 4 + kg + 2) * 1024);
    } else if constexpr (t < 16) {
#pragma unroll
      for (int i = 0; i < 4; ++i) glds16(src_wo[t - 14][i], slot + (4 * w + i) * 1024);
    }
  };
  issue(std::integral_constant<int, 0>{});
  issue(std::integral_constant<int, 1>{});
  issue(std::integral_constant<int, 2>{});

  f32x4 acc1[4], acc2[4];
#pragma unroll
  for (int c = 0; c < 4; ++c) { acc1[c] = f32x4{0.f, 0.f, 0.f, 0.f}; acc2[c] = acc1[c]; }

  int offa[4], offb[4], offbw[4];
#pragma unroll
  for (int c = 0; c < 4; ++c) {
    int r1 = 4 * g + q;
    offa[c] = r1 * 128 + ((c ^ ((r1 >> 1) & 3)) << 5) + 8 * p;
    offb[c] = offa[c] + 16 * 128;
    int r2 = 16 + 4 * (g > 2 ? 2 : g) + q;
    offbw[c] = r2 * 128 + ((c ^ ((r2 >> 1) & 3)) << 5) + 8 * p;
  }

  u32x4 kvf[8][2];
  u32x4 pqf[8];
  const unsigned h_lds = (unsigned)(uintptr_t)(lds_void_ptr)smem + 16384 + (rt * 4 + kg) * 1024 + l * 16;

  static_for<0, 14>([&](auto tc) {
    constexpr int t = tc.value;
    wait_vm<8>();
    bar();
    issue(std::integral_constant<int, t + 3>{});
    const char* slot = smem + (t & 3) * K2_SLOT;
    bf16x8 hf[2];
    asm volatile("ds_read_b128 %0, %2\n\tds_read_b128 %1, %2 offset:2048\n\ts_waitcnt lgkmcnt(0)"
                 : "=&v"(hf[0]), "=&v"(hf[1]) : "v"(h_lds + (t & 3) * K2_SLOT) : "memory");
#pragma unroll
    for (int kk = 0; kk < 2; ++kk) {
      const int ks = kg + 2 * kk;
#pragma unroll
      for (int c = 0; c < 4; ++c) {
        bf16x8 bfg = tr_frag(slot + ks * 4096 + offa[c], slot + ks * 4096 + offb[c]);
        acc1[c] = mfma16(bfg, hf[kk], acc1[c]);
      }
    }
    if constexpr (t == 9) {
      unsigned spins = 0;
      while (__hip_atomic_load((unsigned*)(ws + WS_FLAG), __ATOMIC_RELAXED, __HIP_MEMORY_SCOPE_AGENT) < 16u) {
        __builtin_amdgcn_s_sleep(2);
        if (++spins > (1u << 24)) break;
      }
      asm volatile("" ::: "memory");
      const __amdgpu_buffer_rsrc_t rs = __builtin_amdgcn_make_buffer_rsrc(ws + WS_KVR, 0, 32768, 0x00020000);
#pragma unroll
      for (int hh = 0; hh < 8; ++hh) {
        const int head = 2 * hh + kg;
        kvf[hh][0] = __builtin_amdgcn_raw_buffer_load_b128(rs, (unsigned)(((head * 2 + 0) * 64 + l) * 16), 0, 16);
        kvf[hh][1] = __builtin_amdgcn_raw_buffer_load_b128(rs, (unsigned)(((head * 2 + 1) * 64 + l) * 16), 0, 16);
        pqf[hh] = *(const u32x4*)(ws + WS_PQ + ((size_t)((mt * 4 + rt) * 16 + head) * 64 + l) * 16);
      }
    }
  });

  static_for<14, 16>([&](auto tc) {
    constexpr int t = tc.value;
    if constexpr (t == 14) wait_vm<4>(); else wait_vm<0>();
    bar();
    const char* slot = smem + (t & 3) * K2_SLOT;
#pragma unroll
    for (int i = 0; i < 4; ++i) {
      const int hh = (t - 14) * 4 + i;
      const int hl = 2 * i + kg;
      bf16x8 pq = __builtin_bit_cast(bf16x8, pqf[hh]);
      f32x4 n0v = mfma16(__builtin_bit_cast(bf16x8, kvf[hh][0]), pq, f32x4{0.f, 0.f, 0.f, 0.f});
      f32x4 n1v = mfma16(__builtin_bit_cast(bf16x8, kvf[hh][1]), pq, f32x4{0.f, 0.f, 0.f, 0.f});
      float den = __shfl(n1v[0], (l & 15) + 48);
      den = bfr(bfr(den) + 1e-6f);
      float o[8];
#pragma unroll
      for (int j = 0; j < 4; ++j) {
        o[j] = bfr(n0v[j]) / den;
        float t1 = bfr(n1v[j]) / den;
        o[4 + j] = g == 3 ? 0.f : t1;
      }
      u32x4 ov = {pack2(o[0], o[1]), pack2(o[2], o[3]), pack2(o[4], o[5]), pack2(o[6], o[7])};
      bf16x8 of = __builtin_bit_cast(bf16x8, ov);
      const char* hb = slot + hl * 28 * 128;
#pragma unroll
      for (int c = 0; c < 4; ++c) {
        int r1 = 4 * g + q, r2 = 16 + 4 * (g > 2 ? 2 : g) + q;
        int rho1 = 28 * hl + r1, rho2 = 28 * hl + r2;
        const char* a1 = slot + rho1 * 128 + ((c ^ ((rho1 >> 1) & 3)) << 5) + 8 * p;
        const char* a2 = slot + rho2 * 128 + ((c ^ ((rho2 >> 1) & 3)) << 5) + 8 * p;
        bf16x8 bfg = tr_frag(a1, a2);
        acc2[c] = mfma16(bfg, of, acc2[c]);
      }
      (void)hb;
    }
  });
  (void)offbw;

  bar();
  {
    char* ex = smem + w * 4096;
    const int oc = 2 * (1 - kg);
    *(f32x4*)(ex + 0 * 1024 + l * 16) = kg ? acc1[0] : acc1[2];
    *(f32x4*)(ex + 1 * 1024 + l * 16) = kg ? acc1[1] : acc1[3];
    *(f32x4*)(ex + 2 * 1024 + l * 16) = kg ? acc2[0] : acc2[2];
    *(f32x4*)(ex + 3 * 1024 + l * 16) = kg ? acc2[1] : acc2[3];
    (void)oc;
  }
  bar();
  {
    const char* ex = smem + (w ^ 4) * 4096;
    const int row = mt * 64 + rt * 16 + (l & 15);
#pragma unroll
    for (int i = 0; i < 2; ++i) {
      f32x4 a1 = (kg ? acc1[2 + i] : acc1[i]) + *(const f32x4*)(ex + i * 1024 + l * 16);
      f32x4 a2 = (kg ? acc2[2 + i] : acc2[i]) + *(const f32x4*)(ex + (2 + i) * 1024 + l * 16);
      const int col = n0 + (2 * kg + i) * 16 + 4 * g;
      const u32x2 xr = *(const u32x2*)(x + (size_t)row * DM + col);
      float xv[4] = {bf_lo(xr[0]), bf_hi(xr[0]), bf_lo(xr[1]), bf_hi(xr[1])};
      float r[4];
#pragma unroll
      for (int j = 0; j < 4; ++j) {
        float gg = silu_bf(bfr(a1[j]));
        float s = bfr(a2[j]);
        r[j] = xv[j] + bfr(s * gg);
      }
      u32x2 ov = {pack2(r[0], r[1]), pack2(r[2], r[3])};
      *(u32x2*)(out + (size_t)row * DM + col) = ov;
    }
  }
}

__global__ __launch_bounds__(512, 2) void k2_kernel(const __hip_bfloat16* __restrict__ x,
                                                    const __hip_bfloat16* __restrict__ wo,
                                                    const __hip_bfloat16* __restrict__ w2,
                                                    char* ws, __hip_bfloat16* __restrict__ out) {
  __shared__ __attribute__((aligned(16))) char smem[4 * K2_SLOT];
  const int tid = threadIdx.x, w = __builtin_amdgcn_readfirstlane(tid >> 6), l = tid & 63;
  const int b = blockIdx.x;
  if (b >= K2_NGEMM) {
    const int head = b - K2_NGEMM;
    if (tid < 128) {
      const int et = tid >> 6;
      f32x4 s0 = f32x4{0.f, 0.f, 0.f, 0.f}, s1 = s0;
#pragma unroll
      for (int m = 0; m < 16; ++m) {
        const char* pb = ws + WS_KVP + ((size_t)(m * 16 + head) * 256) * 16;
        s0 += *(const f32x4*)(pb + ((0 * 2 + et) * 64 + l) * 16);
        s1 += *(const f32x4*)(pb + ((1 * 2 + et) * 64 + l) * 16);
      }
      if ((l >> 4) == 3) s1 = f32x4{0.f, 0.f, 0.f, 0.f};
      u32x4 o = {pack2(s0[0], s0[1]), pack2(s0[2], s0[3]), pack2(s1[0], s1[1]), pack2(s1[2], s1[3])};
      const __amdgpu_buffer_rsrc_t rs = __builtin_amdgcn_make_buffer_rsrc(ws + WS_KVR, 0, 32768, 0x00020000);
      __builtin_amdgcn_raw_buffer_store_b128(o, rs, (unsigned)(((head * 2 + et) * 64 + l) * 16), 0, 16);
      asm volatile("s_waitcnt vmcnt(0)" ::: "memory");
    }
    __syncthreads();
    if (tid == 0)
      __hip_atomic_fetch_add((unsigned*)(ws + WS_FLAG), 1u, __ATOMIC_RELAXED, __HIP_MEMORY_SCOPE_AGENT);
    return;
  }
  k2_gemm(smem, x, wo, w2, ws, out);
}

extern "C" void kernel_launch(void* const* d_in, const int* in_sizes, int n_in, void* d_out, int out_size,
                              void* d_ws, size_t ws_size, hipStream_t stream) {
  const __hip_bfloat16* x = (const __hip_bfloat16*)d_in[0];
  const __hip_bfloat16* wqkv = (const __hip_bfloat16*)d_in[1];
  const __hip_bfloat16* wo = (const __hip_bfloat16*)d_in[2];
  const __hip_bfloat16* w1 = (const __hip_bfloat16*)d_in[4];
  const __hip_bfloat16* w3 = (const __hip_bfloat16*)d_in[5];
  const __hip_bfloat16* w2 = (const __hip_bfloat16*)d_in[6];
  char* ws = (char*)d_ws;
  k1_kernel<<<256, 512, 0, stream>>>(x, wqkv, w1, w3, ws);
  k2_kernel<<<K2_NGEMM + 16, 512, 0, stream>>>(x, wo, w2, ws, (__hip_bfloat16*)d_out);
}
```

```cpp
#include <hip/hip_runtime.h>
#include <hip/hip_bf16.h>
#include <stdint.h>
#include <type_traits>

typedef __attribute__((ext_vector_type(8))) __bf16 bf16x8;
typedef __attribute__((ext_vector_type(4))) short s16x4;
typedef __attribute__((ext_vector_type(8))) short s16x8;
typedef __attribute__((ext_vector_type(4))) float f32x4;
typedef __attribute__((ext_vector_type(2))) float f32x2;
typedef __attribute__((ext_vector_type(4))) unsigned u32x4;
typedef __attribute__((ext_vector_type(2))) unsigned u32x2;
typedef __attribute__((address_space(3))) s16x4* lds_s16x4_ptr;
typedef __attribute__((address_space(3))) void* lds_void_ptr;
typedef const __attribute__((address_space(1))) void* gbl_cvoid_ptr;

constexpr int SEQ = 2048, DM = 448, FF = 1792, NH = 16, DH = 28;

constexpr size_t WS_H    = 0;
constexpr size_t WS_PQ   = 7340032;
constexpr size_t WS_KVP  = WS_PQ + 2097152;
constexpr size_t WS_KVR  = WS_KVP + 1048576;
constexpr size_t WS_FLAG = WS_KVR + 32768;

#define STAMP(slot) do{}while(0)

template <int I, int N, class Fn>
__device__ __forceinline__ void static_for(Fn&& f) {
  if constexpr (I < N) { f(std::integral_constant<int, I>{}); static_for<I + 1, N>(f); }
}
template <int N> using ic = std::integral_constant<int, N>;
template <int N> __device__ __forceinline__ void wait_vm() {
  asm volatile("" ::: "memory");
  __builtin_amdgcn_s_waitcnt((N & 0xF) | (7 << 4) | (0xF << 8) | ((N >> 4) << 14));
  asm volatile("" ::: "memory");
}
__device__ __forceinline__ void bar() {
  asm volatile("" ::: "memory");
  __builtin_amdgcn_s_waitcnt(0xC07F);
  __builtin_amdgcn_s_barrier();
  asm volatile("" ::: "memory");
}

__device__ __forceinline__ float bfr(float f) { return (float)(__bf16)f; }
__device__ __forceinline__ float bf_lo(unsigned u) { return __uint_as_float(u << 16); }
__device__ __forceinline__ float bf_hi(unsigned u) { return __uint_as_float(u & 0xffff0000u); }
__device__ __forceinline__ unsigned pack2(float a, float b) {
  typedef __attribute__((ext_vector_type(2))) __bf16 bf16x2;
  bf16x2 v; v[0] = (__bf16)a; v[1] = (__bf16)b;
  return __builtin_bit_cast(unsigned, v);
}
__device__ __forceinline__ void glds16(const void* g, void* l) {
  __builtin_amdgcn_global_load_lds((gbl_cvoid_ptr)g, (lds_void_ptr)l, 16, 0, 0);
}
__device__ __forceinline__ bf16x8 tr_frag(const char* lds_a, const char* lds_b) {
  s16x4 lo = __builtin_amdgcn_ds_read_tr16_b64_v4i16((lds_s16x4_ptr)lds_a);
  s16x4 hi = __builtin_amdgcn_ds_read_tr16_b64_v4i16((lds_s16x4_ptr)lds_b);
  s16x8 v = __builtin_shufflevector(lo, hi, 0, 1, 2, 3, 4, 5, 6, 7);
  return __builtin_bit_cast(bf16x8, v);
}
__device__ __forceinline__ f32x4 mfma16(bf16x8 a, bf16x8 b, f32x4 c) {
  return __builtin_amdgcn_mfma_f32_16x16x32_bf16(a, b, c, 0, 0, 0);
}
__device__ __forceinline__ float rcp_(float v) { return __builtin_amdgcn_rcpf(v); }
__device__ __forceinline__ float exp_(float v) { return __builtin_amdgcn_exp2f(v * 1.44269504088896f); }
__device__ __forceinline__ float silu_(float v) { return v * rcp_(1.0f + exp_(-v)); }
__device__ __forceinline__ float phi_(float t) { return (t > 0.0f ? t + 1.0f : exp_(t)) + 1e-6f; }

constexpr int K1_SLOT = 24576;
constexpr int K1_XN_BYTES = 114688;
__host__ __device__ constexpr int k1_slot_off(int s) { return s < 2 ? K1_XN_BYTES + K1_SLOT * s : K1_SLOT * (s - 2); }
__host__ __device__ constexpr int k1_seg_kind(int type, int seg) {
  return type == 0 ? (seg == 0 ? 0 : 1) : (seg < 2 ? 1 : 2);
}
__host__ __device__ constexpr int k1_nstage(int type) { return type == 0 ? 14 : 21; }
__host__ __device__ constexpr int k1_cnt(int type, int t) {
  return t >= k1_nstage(type) ? 0 : (k1_seg_kind(type, t / 7) == 0 ? 3 : (k1_seg_kind(type, t / 7) == 1 ? 2 : 1));
}

template <int TYPE>
__device__ __forceinline__ void k1_body(char* smem, const __hip_bfloat16* __restrict__ x,
                                        const __hip_bfloat16* __restrict__ wqkv,
                                        const __hip_bfloat16* __restrict__ w1,
                                        const __hip_bfloat16* __restrict__ w3, char* ws, int mtile,
                                        int role) {
  const int tid = threadIdx.x, w = __builtin_amdgcn_readfirstlane(tid >> 6), l = tid & 63;
  const int rg = w & 3, cg = w >> 2;
  const int g = l >> 4, q = (l & 15) >> 2, p = l & 3;
  constexpr int NSEG = TYPE == 0 ? 2 : 3;
  constexpr int NST = k1_nstage(TYPE);
  constexpr int STAMP_K = 0; STAMP(0);

  const char* src[NSEG][3];
  const int unit0 = TYPE == 0 ? role : 8 + 2 * (role - 8);
  static_for<0, NSEG>([&](auto sc) {
    constexpr int seg = sc.value;
    constexpr int kind = k1_seg_kind(TYPE, seg);
    if constexpr (kind == 1) {
      int n0 = (TYPE == 0 ? unit0 : unit0 + seg) * 64;
      int f = (l >> 4) | ((w & 1) << 2);
      int ch = (l & 15) ^ (f << 1);
      const __hip_bfloat16* base = ch < 8 ? w1 : w3;
      for (int b = 0; b < 2; ++b) {
        int row = 8 * w + 4 * b + (l >> 4);
        src[seg][b] = (const char*)(base + (size_t)row * FF + n0 + 8 * (ch & 7));
      }
    } else if constexpr (kind == 2) {
      int n0 = 1536 + 32 * (role - 8);
      int rr = l >> 3;
      int key = ((rr >> 1) & 1) | ((w & 1) << 1);
      int ch = (l & 7) ^ (key << 1);
      const __hip_bfloat16* base = ch < 4 ? w1 : w3;
      int row = 8 * w + rr;
      src[seg][0] = (const char*)(base + (size_t)row * FF + n0 + 8 * (ch & 3));
    } else {
      for (int b = 0; b < 3; ++b) {
        int cl = 64 * (3 * w + b) + l;
        int row = cl / 24, chp = cl % 24;
        int blk = chp >> 3;
        int key = ((row >> 1) & 1) | (((row >> 3) & 1) << 1);
        int cc = (chp & 7) ^ (key << 1);
        int head = 2 * role + (cc >> 2);
        int col = blk * DM + DH * head + 8 * (cc & 3);
        if (blk == 2 && head == 15) col -= 4;
        src[seg][b] = (const char*)(wqkv + (size_t)row * (3 * DM) + col);
      }
    }
  });

  auto issue = [&](auto tc) {
    constexpr int t = tc.value;
    if constexpr (t < NST) {
      constexpr int seg = t / 7, s = t % 7, kind = k1_seg_kind(TYPE, seg);
      char* slot = smem + k1_slot_off(t % 6);
      if constexpr (kind == 1) {
        glds16(src[seg][0] + (size_t)s * 64 * FF * 2, slot + (8 * w) * 256);
        glds16(src[seg][1] + (size_t)s * 64 * FF * 2, slot + (8 * w + 4) * 256);
      } else if constexpr (kind == 2) {
        glds16(src[seg][0] + (size_t)s * 64 * FF * 2, slot + w * 1024);
      } else {
        glds16(src[seg][0] + (size_t)s * 64 * (3 * DM) * 2, slot + (3 * w) * 1024);
        glds16(src[seg][1] + (size_t)s * 64 * (3 * DM) * 2, slot + (3 * w + 1) * 1024);
        glds16(src[seg][2] + (size_t)s * 64 * (3 * DM) * 2, slot + (3 * w + 2) * 1024);
      }
    }
  };

  {
    const int row = l >> 2, sub = l & 3;
    const char* xr = (const char*)(x + (size_t)(mtile * 128 + 16 * w + row) * DM) + sub * 16;
    u32x4 raw[14];
#pragma unroll
    for (int i = 0; i < 14; ++i) raw[i] = *(const u32x4*)(xr + i * 64);
    asm volatile("" ::: "memory");
    issue(ic<0>{});
    issue(ic<1>{});
    f32x2 d[56];
    f32x2 sv = f32x2{0.f, 0.f};
#pragma unroll
    for (int i = 0; i < 14; ++i)
#pragma unroll
      for (int j = 0; j < 4; ++j) {
        d[i * 4 + j] = f32x2{bf_lo(raw[i][j]), bf_hi(raw[i][j])};
        sv += d[i * 4 + j];
      }
    float sum = sv[0] + sv[1];
    sum += __shfl_xor(sum, 1); sum += __shfl_xor(sum, 2);
    const float mean = sum * (1.0f / DM);
    const f32x2 mean2 = f32x2{mean, mean};
    f32x2 vv2 = f32x2{0.f, 0.f};
#pragma unroll
    for (int i = 0; i < 56; ++i) { d[i] -= mean2; vv2 += d[i] * d[i]; }
    float var = vv2[0] + vv2[1];
    var += __shfl_xor(var, 1); var += __shfl_xor(var, 2);
    var = bfr(var * (1.0f / DM));
    const float den = bfr(sqrtf(bfr(var + 1e-5f)));
    const float inv = 1.0f / den;
    const float c0 = (mean - bfr(mean)) * inv;
    const f32x2 inv2 = f32x2{inv, inv}, c02 = f32x2{c0, c0};
    char* dst = smem + w * 14336 + (((row ^ (2 * sub)) + 16 * sub) << 4);
#pragma unroll
    for (int i = 0; i < 14; ++i) {
      u32x4 o;
#pragma unroll
      for (int j = 0; j < 4; ++j) {
        f32x2 r = d[i * 4 + j] * inv2 + c02;
        o[j] = pack2(r[0], r[1]);
      }
      *(u32x4*)(dst + i * 1024) = o;
    }
  }
  STAMP(1);
  bar();
  bf16x8 af[2][14];
  {
    const int r = l & 15;
    const char* s0 = smem + (2 * rg) * 14336 + (((r ^ (2 * g)) + 16 * g) << 4);
#pragma unroll
    for (int rt = 0; rt < 2; ++rt)
#pragma unroll
      for (int i = 0; i < 14; ++i) af[rt][i] = *(const bf16x8*)(s0 + rt * 14336 + i * 1024);
  }
  bar();
  issue(ic<2>{});
  issue(ic<3>{});
  issue(ic<4>{});
  STAMP(2);

  const int rt16 = mtile * 8 + rg * 2;
  const int f_ffn = q | ((g & 1) << 2);
  const int key2 = ((q >> 1) & 1) | ((g & 1) << 1);
  const int head = 2 * role + cg;
  int off1[4], off2[4], off0[6];
#pragma unroll
  for (int c = 0; c < 4; ++c) {
    int tile = (c >> 1) * 4 + 2 * cg + (c & 1);
    off1[c] = (8 * g + q) * 256 + ((tile ^ f_ffn) << 5) + 8 * p;
    off2[c] = (8 * g + q) * 128 + ((c ^ key2) << 5) + 8 * p;
  }
#pragma unroll
  for (int c = 0; c < 6; ++c) {
    int blk = c >> 1, ct = c & 1;
    int pi = 4 * ct + p + ((blk == 2 && head == 15) ? 1 : 0);
    pi = pi > 7 ? 7 : pi;
    int cc = 4 * cg + (pi >> 1);
    off0[c] = (8 * g + q) * 384 + blk * 128 + ((cc ^ (key2 << 1)) << 4) + 8 * (pi & 1);
  }

  bf16x8 bb[2][4];
  f32x4 acc[2][6];
  auto ldB = [&](auto tc, auto pc, bf16x8* dst) {
    constexpr int t = tc.value, ph = pc.value;
    constexpr int kind = k1_seg_kind(TYPE, t / 7);
    const char* slot = smem + k1_slot_off(t % 6);
    if constexpr (kind == 1) {
#pragma unroll
      for (int c = 0; c < 4; ++c) dst[c] = tr_frag(slot + ph * 8192 + off1[c], slot + ph * 8192 + 1024 + off1[c]);
    } else if constexpr (kind == 2) {
      if (cg == 0) {
#pragma unroll
        for (int c = 0; c < 4; ++c) dst[c] = tr_frag(slot + ph * 4096 + off2[c], slot + ph * 4096 + 512 + off2[c]);
      }
    } else {
      constexpr int ks = ph >> 1, c0 = 3 * (ph & 1);
#pragma unroll
      for (int c = 0; c < 3; ++c)
        dst[c] = tr_frag(slot + ks * 12288 + off0[c0 + c], slot + ks * 12288 + 1536 + off0[c0 + c]);
    }
  };
  auto mma = [&](auto tc, auto pc, const bf16x8* b) {
    constexpr int t = tc.value, ph = pc.value, s = t % 7;
    constexpr int kind = k1_seg_kind(TYPE, t / 7);
    const f32x4 z = f32x4{0.f, 0.f, 0.f, 0.f};
    if constexpr (kind == 1) {
      constexpr bool first = (s == 0 && ph == 0);
#pragma unroll
      for (int c = 0; c < 4; ++c) {
        acc[0][c] = mfma16(b[c], af[0][2 * s + ph], first ? z : acc[0][c]);
        acc[1][c] = mfma16(b[c], af[1][2 * s + ph], first ? z : acc[1][c]);
      }
    } else if constexpr (kind == 2) {
      constexpr bool first = (s == 0 && ph == 0);
      if (cg == 0) {
#pragma unroll
        for (int c = 0; c < 4; ++c) {
          acc[0][c] = mfma16(b[c], af[0][2 * s + ph], first ? z : acc[0][c]);
          acc[1][c] = mfma16(b[c], af[1][2 * s + ph], first ? z : acc[1][c]);
        }
      }
    } else {
      constexpr int ks = ph >> 1, c0 = 3 * (ph & 1);
      constexpr bool first = (s == 0 && ks == 0);
#pragma unroll
      for (int c = 0; c < 3; ++c) {
        if (c0 + c < 2) {
          acc[0][c0 + c] = mfma16(b[c], af[0][2 * s + ks], first ? z : acc[0][c0 + c]);
          acc[1][c0 + c] = mfma16(b[c], af[1][2 * s + ks], first ? z : acc[1][c0 + c]);
        } else {
          acc[0][c0 + c] = mfma16(af[0][2 * s + ks], b[c], first ? z : acc[0][c0 + c]);
          acc[1][c0 + c] = mfma16(af[1][2 * s + ks], b[c], first ? z : acc[1][c0 + c]);
        }
      }
    }
  };
  auto ffn_epi = [&](int kstep) {
#pragma unroll
    for (int rt = 0; rt < 2; ++rt) {
      float h[8];
#pragma unroll
      for (int c = 0; c < 2; ++c)
#pragma unroll
        for (int j = 0; j < 4; ++j) h[c * 4 + j] = silu_(acc[rt][c][j]) * acc[rt][2 + c][j];
      u32x4 o = {pack2(h[0], h[1]), pack2(h[2], h[3]), pack2(h[4], h[5]), pack2(h[6], h[7])};
      *(u32x4*)(ws + WS_H + ((size_t)((rt16 + rt) * 56 + kstep) * 64 + l) * 16) = o;
    }
  };

  wait_vm<k1_cnt(TYPE, 1) + k1_cnt(TYPE, 2) + k1_cnt(TYPE, 3) + k1_cnt(TYPE, 4)>();
  bar();
  ldB(ic<0>{}, ic<0>{}, bb[0]);

  static_for<0, NST>([&](auto tc) {
    constexpr int t = tc.value;
    constexpr int seg = t / 7, kind = k1_seg_kind(TYPE, seg);
    wait_vm<k1_cnt(TYPE, t + 2) + k1_cnt(TYPE, t + 3) + k1_cnt(TYPE, t + 4)>();
    bar(); STAMP(3 + t);
    issue(ic<t + 5>{});
    constexpr int NPH = kind == 0 ? 4 : 2;
    static_for<0, NPH>([&](auto pc) {
      constexpr int ph = pc.value;
      if constexpr (ph > 0) __builtin_amdgcn_s_waitcnt(0xC07F);
      if constexpr (ph + 1 < NPH) ldB(tc, ic<ph + 1>{}, bb[(ph + 1) & 1]);
      else if constexpr (t + 1 < NST) ldB(ic<t + 1>{}, ic<0>{}, bb[0]);
      __builtin_amdgcn_sched_barrier(0);
      mma(tc, pc, bb[ph & 1]);
      __builtin_amdgcn_sched_barrier(0);
    });
    if constexpr (t % 7 == 6) {
      if constexpr (kind == 1) {
        ffn_epi((TYPE == 0 ? unit0 : unit0 + seg) * 2 + cg);
      } else if constexpr (kind == 2) {
        if (cg == 0) ffn_epi(48 + (role - 8));
      } else {
#pragma unroll
        for (int rt = 0; rt < 2; ++rt) {
          float v[8];
#pragma unroll
          for (int j = 0; j < 4; ++j) {
            v[j] = phi_(acc[rt][0][j]);
            float t1 = phi_(acc[rt][1][j]);
            v[4 + j] = g == 3 ? 0.f : t1;
          }
          u32x4 o = {pack2(v[0], v[1]), pack2(v[2], v[3]), pack2(v[4], v[5]), pack2(v[6], v[7])};
          *(u32x4*)(ws + WS_PQ + ((size_t)((rt16 + rt) * 16 + head) * 64 + l) * 16) = o;
        }
        bf16x8 pk[2], vv[2];
#pragma unroll
        for (int ct = 0; ct < 2; ++ct) {
          u32x4 a = {pack2(phi_(acc[0][2 + ct][0]), phi_(acc[0][2 + ct][1])),
                     pack2(phi_(acc[0][2 + ct][2]), phi_(acc[0][2 + ct][3])),
                     pack2(phi_(acc[1][2 + ct][0]), phi_(acc[1][2 + ct][1])),
                     pack2(phi_(acc[1][2 + ct][2]), phi_(acc[1][2 + ct][3]))};
          pk[ct] = __builtin_bit_cast(bf16x8, a);
          u32x4 b = {pack2(acc[0][4 + ct][0], acc[0][4 + ct][1]), pack2(acc[0][4 + ct][2], acc[0][4 + ct][3]),
                     pack2(acc[1][4 + ct][0], acc[1][4 + ct][1]), pack2(acc[1][4 + ct][2], acc[1][4 + ct][3])};
          if (ct == 1 && (l & 15) == 12) b = u32x4{0x3f803f80u, 0x3f803f80u, 0x3f803f80u, 0x3f803f80u};
          vv[ct] = __builtin_bit_cast(bf16x8, b);
        }
        f32x4 kvd[4];
#pragma unroll
        for (int dt = 0; dt < 2; ++dt)
#pragma unroll
          for (int et = 0; et < 2; ++et) kvd[dt * 2 + et] = mfma16(pk[dt], vv[et], f32x4{0.f, 0.f, 0.f, 0.f});
        bar();
        {
          char* sc = smem + k1_slot_off(w >> 1) + 16384 + (w & 1) * 4096;
#pragma unroll
          for (int i = 0; i < 4; ++i) *(f32x4*)(sc + i * 1024 + l * 16) = kvd[i];
        }
        bar();
        {
          const int hh = tid >> 8, idx = tid & 255;
          f32x4 sum = f32x4{0.f, 0.f, 0.f, 0.f};
#pragma unroll
          for (int r = 0; r < 4; ++r) {
            int ww = hh * 4 + r;
            sum += *(const f32x4*)(smem + k1_slot_off(ww >> 1) + 16384 + (ww & 1) * 4096 + idx * 16);
          }
          *(f32x4*)(ws + WS_KVP + ((size_t)(mtile * 16 + 2 * role + hh) * 256 + idx) * 16) = sum;
        }
      }
    }
  });
  STAMP(31);
}

__global__ __launch_bounds__(512, 2) void k1_kernel(const __hip_bfloat16* __restrict__ x,
                                                    const __hip_bfloat16* __restrict__ wqkv,
                                                    const __hip_bfloat16* __restrict__ w1,
                                                    const __hip_bfloat16* __restrict__ w3, char* ws) {
  __shared__ __attribute__((aligned(16))) char smem[163840];
  const int b = blockIdx.x;
  const int role = 2 * (b & 7) + ((b >> 3) & 1), mtile = b >> 4;
  if (b == 0 && threadIdx.x == 0) *(unsigned*)(ws + WS_FLAG) = 0u;
  if (role < 8) k1_body<0>(smem, x, wqkv, w1, w3, ws, mtile, role);
  else k1_body<1>(smem, x, wqkv, w1, w3, ws, mtile, role);
}

constexpr int K2_SLOT = 32768;
constexpr int K2_NSLOT = 5;
constexpr int K2_NGEMM = 224;

__device__ __forceinline__ void k2_gemm(char* smem, const __hip_bfloat16* __restrict__ x,
                                        const __hip_bfloat16* __restrict__ wo,
                                        const __hip_bfloat16* __restrict__ w2, char* ws,
                                        __hip_bfloat16* __restrict__ out) {
  const int tid = threadIdx.x, w = __builtin_amdgcn_readfirstlane(tid >> 6), l = tid & 63;
  const int b = blockIdx.x;
  const int xcd = b & 7, idx = b >> 3;
  const int mt = 4 * xcd + idx / 7, nt = idx % 7;
  const int rt = w & 3, kg = w >> 2;
  const int g = l >> 4, q = (l & 15) >> 2, p = l & 3;
  const int n0 = nt * 64;
  constexpr int STAMP_K = 1; STAMP(0);

  u32x4 pqf[8];
#pragma unroll
  for (int hh = 0; hh < 8; ++hh)
    pqf[hh] = *(const u32x4*)(ws + WS_PQ + ((size_t)((mt * 4 + rt) * 16 + 2 * hh + kg) * 64 + l) * 16);
  const int orow = mt * 64 + rt * 16 + (l & 15);
  u32x2 xr[2];
#pragma unroll
  for (int i = 0; i < 2; ++i) xr[i] = *(const u32x2*)(x + (size_t)orow * DM + n0 + (2 * kg + i) * 16 + 4 * g);
  asm volatile("" ::: "memory");

  const char* src_w2[2];
  const char* src_wo[2][4];
  {
    int chs = (l & 7) ^ (((l >> 4) & 3) << 1);
    for (int i = 0; i < 2; ++i) {
      int row = 8 * (2 * w + i) + (l >> 3);
      src_w2[i] = (const char*)(w2 + (size_t)row * DM + n0 + 8 * chs);
    }
    for (int st = 0; st < 2; ++st)
      for (int i = 0; i < 4; ++i) {
        int row = 224 * st + 8 * (4 * w + i) + (l >> 3);
        row = row > 447 ? 447 : row;
        src_wo[st][i] = (const char*)(wo + (size_t)row * DM + n0 + 8 * chs);
      }
  }
  const char* src_h = ws + WS_H + ((size_t)((mt * 4 + rt) * 56 + kg) * 64 + l) * 16;

  auto issue = [&](auto tc) {
    constexpr int t = tc.value;
    char* slot = smem + (t % K2_NSLOT) * K2_SLOT;
    if constexpr (t < 14) {
      glds16(src_w2[0] + (size_t)t * 128 * DM * 2, slot + (2 * w) * 1024);
      glds16(src_w2[1] + (size_t)t * 128 * DM * 2, slot + (2 * w + 1) * 1024);
      glds16(src_h + (size_t)(t * 4) * 1024, slot + 16384 + (rt * 4 + kg) * 1024);
      glds16(src_h + (size_t)(t * 4 + 2) * 1024, slot + 16384 + (rt * 4 + kg + 2) * 1024);
    } else if constexpr (t < 16) {
#pragma unroll
      for (int i = 0; i < 4; ++i) glds16(src_wo[t - 14][i], slot + (4 * w + i) * 1024);
    }
  };
  static_for<0, K2_NSLOT - 1>([&](auto tc) { issue(tc); });

  f32x4 acc1[4], acc2[4];
#pragma unroll
  for (int c = 0; c < 4; ++c) { acc1[c] = f32x4{0.f, 0.f, 0.f, 0.f}; acc2[c] = acc1[c]; }

  int offa[4], offwa[4], offwb[4];
#pragma unroll
  for (int c = 0; c < 4; ++c) {
    int r1 = 4 * g + q;
    offa[c] = r1 * 128 + ((c ^ ((r1 >> 1) & 3)) << 5) + 8 * p;
    int r2 = 16 + 4 * (g > 2 ? 2 : g) + q;
    offwa[c] = (28 * kg + r1) * 128 + ((c ^ ((2 * kg + (r1 >> 1)) & 3)) << 5) + 8 * p;
    offwb[c] = (28 * kg + r2) * 128 + ((c ^ ((2 * kg + (r2 >> 1)) & 3)) << 5) + 8 * p;
  }

  u32x4 kvf[8][2];
  const unsigned h_lds = (unsigned)(uintptr_t)(lds_void_ptr)smem + 16384 + (rt * 4 + kg) * 1024 + l * 16;

  static_for<0, 14>([&](auto tc) {
    constexpr int t = tc.value;
    wait_vm<4 * ((K2_NSLOT - 2) < (15 - t) ? (K2_NSLOT - 2) : (15 - t))>();
    bar(); STAMP(1 + t);
    issue(ic<t + K2_NSLOT - 1>{});
    if constexpr (t == 13) {
      unsigned spins = 0;
      while (__hip_atomic_load((unsigned*)(ws + WS_FLAG), __ATOMIC_RELAXED, __HIP_MEMORY_SCOPE_AGENT) < 16u) {
        __builtin_amdgcn_s_sleep(2);
        if (++spins > (1u << 24)) break;
      }
      asm volatile("" ::: "memory");
      const __amdgpu_buffer_rsrc_t rs = __builtin_amdgcn_make_buffer_rsrc(ws + WS_KVR, 0, 32768, 0x00020000);
#pragma unroll
      for (int hh = 0; hh < 8; ++hh) {
        const int head = 2 * hh + kg;
        kvf[hh][0] = __builtin_amdgcn_raw_buffer_load_b128(rs, (unsigned)(((head * 2 + 0) * 64 + l) * 16), 0, 16);
        kvf[hh][1] = __builtin_amdgcn_raw_buffer_load_b128(rs, (unsigned)(((head * 2 + 1) * 64 + l) * 16), 0, 16);
      }
    }
    const char* slot = smem + (t % K2_NSLOT) * K2_SLOT;
    bf16x8 hf[2];
    asm volatile("ds_read_b128 %0, %2\n\tds_read_b128 %1, %2 offset:2048\n\ts_waitcnt lgkmcnt(0)"
                 : "=&v"(hf[0]), "=&v"(hf[1]) : "v"(h_lds + (t % K2_NSLOT) * K2_SLOT) : "memory");
#pragma unroll
    for (int kk = 0; kk < 2; ++kk) {
      const int ks = kg + 2 * kk;
#pragma unroll
      for (int c = 0; c < 4; ++c) {
        bf16x8 bfg = tr_frag(slot + ks * 4096 + offa[c], slot + ks * 4096 + 2048 + offa[c]);
        acc1[c] = mfma16(bfg, hf[kk], acc1[c]);
      }
    }
  });

  bf16x8 of[8];
  {
    f32x4 n0v[8], n1v[8];
#pragma unroll
    for (int hh = 0; hh < 8; ++hh) {
      bf16x8 pq = __builtin_bit_cast(bf16x8, pqf[hh]);
      n0v[hh] = mfma16(__builtin_bit_cast(bf16x8, kvf[hh][0]), pq, f32x4{0.f, 0.f, 0.f, 0.f});
      n1v[hh] = mfma16(__builtin_bit_cast(bf16x8, kvf[hh][1]), pq, f32x4{0.f, 0.f, 0.f, 0.f});
    }
    float den[8];
#pragma unroll
    for (int hh = 0; hh < 8; ++hh) den[hh] = __shfl(n1v[hh][0], (l & 15) + 48);
#pragma unroll
    for (int hh = 0; hh < 8; ++hh) {
      const float inv = rcp_(den[hh] + 1e-6f);
      const float invm = g == 3 ? 0.f : inv;
      u32x4 ov = {pack2(n0v[hh][0] * inv, n0v[hh][1] * inv), pack2(n0v[hh][2] * inv, n0v[hh][3] * inv),
                  pack2(n1v[hh][0] * invm, n1v[hh][1] * invm), pack2(n1v[hh][2] * invm, n1v[hh][3] * invm)};
      of[hh] = __builtin_bit_cast(bf16x8, ov);
    }
  }
  STAMP(20);
  wait_vm<0>();
  bar();
  static_for<14, 16>([&](auto tc) {
    constexpr int t = tc.value;
    STAMP(1 + t);
    const char* slot = smem + (t % K2_NSLOT) * K2_SLOT;
#pragma unroll
    for (int i = 0; i < 4; ++i) {
      const int hh = (t - 14) * 4 + i;
#pragma unroll
      for (int c = 0; c < 4; ++c) {
        bf16x8 bfg = tr_frag(slot + i * 7168 + offwa[c], slot + i * 7168 + offwb[c]);
        acc2[c] = mfma16(bfg, of[hh], acc2[c]);
      }
    }
  });

  STAMP(17);
  {
    char* ex = smem + K2_SLOT + w * 4096;
    *(f32x4*)(ex + 0 * 1024 + l * 16) = kg ? acc1[0] : acc1[2];
    *(f32x4*)(ex + 1 * 1024 + l * 16) = kg ? acc1[1] : acc1[3];
    *(f32x4*)(ex + 2 * 1024 + l * 16) = kg ? acc2[0] : acc2[2];
    *(f32x4*)(ex + 3 * 1024 + l * 16) = kg ? acc2[1] : acc2[3];
  }
  bar();
  {
    const char* ex = smem + K2_SLOT + (w ^ 4) * 4096;
#pragma unroll
    for (int i = 0; i < 2; ++i) {
      f32x4 a1 = (kg ? acc1[2 + i] : acc1[i]) + *(const f32x4*)(ex + i * 1024 + l * 16);
      f32x4 a2 = (kg ? acc2[2 + i] : acc2[i]) + *(const f32x4*)(ex + (2 + i) * 1024 + l * 16);
      const int col = n0 + (2 * kg + i) * 16 + 4 * g;
      float xv[4] = {bf_lo(xr[i][0]), bf_hi(xr[i][0]), bf_lo(xr[i][1]), bf_hi(xr[i][1])};
      float r[4];
#pragma unroll
      for (int j = 0; j < 4; ++j) r[j] = xv[j] + bfr(a2[j]) * bfr(silu_(bfr(a1[j])));
      u32x2 ov = {pack2(r[0], r[1]), pack2(r[2], r[3])};
      *(u32x2*)(out + (size_t)orow * DM + col) = ov;
    }
  }
  STAMP(18);
}

__global__ __launch_bounds__(512, 2) void k2_kernel(const __hip_bfloat16* __restrict__ x,
                                                    const __hip_bfloat16* __restrict__ wo,
                                                    const __hip_bfloat16* __restrict__ w2,
                                                    char* ws, __hip_bfloat16* __restrict__ out) {
  __shared__ __attribute__((aligned(16))) char smem[K2_NSLOT * K2_SLOT];
  const int tid = threadIdx.x, l = tid & 63;
  const int b = blockIdx.x;
  if (b >= K2_NGEMM) {
    const int head = b - K2_NGEMM;
    if (tid < 128) {
      const int et = tid >> 6;
      f32x4 s0 = f32x4{0.f, 0.f, 0.f, 0.f}, s1 = s0;
#pragma unroll
      for (int m = 0; m < 16; ++m) {
        const char* pb = ws + WS_KVP + ((size_t)(m * 16 + head) * 256) * 16;
        s0 += *(const f32x4*)(pb + ((0 * 2 + et) * 64 + l) * 16);
        s1 += *(const f32x4*)(pb + ((1 * 2 + et) * 64 + l) * 16);
      }
      if ((l >> 4) == 3) s1 = f32x4{0.f, 0.f, 0.f, 0.f};
      if (et == 1 && (l & 15) > 12) { s0 = f32x4{0.f, 0.f, 0.f, 0.f}; s1 = s0; }
      u32x4 o = {pack2(s0[0], s0[1]), pack2(s0[2], s0[3]), pack2(s1[0], s1[1]), pack2(s1[2], s1[3])};
      const __amdgpu_buffer_rsrc_t rs = __builtin_amdgcn_make_buffer_rsrc(ws + WS_KVR, 0, 32768, 0x00020000);
      __builtin_amdgcn_raw_buffer_store_b128(o, rs, (unsigned)(((head * 2 + et) * 64 + l) * 16), 0, 16);
      asm volatile("s_waitcnt vmcnt(0)" ::: "memory");
    }
    __syncthreads();
    if (tid == 0)
      __hip_atomic_fetch_add((unsigned*)(ws + WS_FLAG), 1u, __ATOMIC_RELAXED, __HIP_MEMORY_SCOPE_AGENT);
    return;
  }
  k2_gemm(smem, x, wo, w2, ws, out);
}

extern "C" void kernel_launch(void* const* d_in, const int* in_sizes, int n_in, void* d_out, int out_size,
                              void* d_ws, size_t ws_size, hipStream_t stream) {
  const __hip_bfloat16* x = (const __hip_bfloat16*)d_in[0];
  const __hip_bfloat16* wqkv = (const __hip_bfloat16*)d_in[1];
  const __hip_bfloat16* wo = (const __hip_bfloat16*)d_in[2];
  const __hip_bfloat16* w1 = (const __hip_bfloat16*)d_in[4];
  const __hip_bfloat16* w3 = (const __hip_bfloat16*)d_in[5];
  const __hip_bfloat16* w2 = (const __hip_bfloat16*)d_in[6];
  char* ws = (char*)d_ws;
  k1_kernel<<<256, 512, 0, stream>>>(x, wqkv, w1, w3, ws);
  k2_kernel<<<K2_NGEMM + 16, 512, 0, stream>>>(x, wo, w2, ws, (__hip_bfloat16*)d_out);
}
```

```cpp
#include <hip/hip_runtime.h>
#include <hip/hip_bf16.h>
#include <stdint.h>
#include <type_traits>

typedef __attribute__((ext_vector_type(8))) __bf16 bf16x8;
typedef __attribute__((ext_vector_type(4))) short s16x4;
typedef __attribute__((ext_vector_type(8))) short s16x8;
typedef __attribute__((ext_vector_type(4))) float f32x4;
typedef __attribute__((ext_vector_type(2))) float f32x2;
typedef __attribute__((ext_vector_type(4))) unsigned u32x4;
typedef __attribute__((ext_vector_type(2))) unsigned u32x2;
typedef __attribute__((address_space(3))) s16x4* lds_s16x4_ptr;
typedef __attribute__((address_space(3))) void* lds_void_ptr;
typedef const __attribute__((address_space(1))) void* gbl_cvoid_ptr;

constexpr int SEQ = 2048, DM = 448, FF = 1792, NH = 16, DH = 28;

constexpr size_t WS_H    = 0;
constexpr size_t WS_PQ   = 7340032;
constexpr size_t WS_KVP  = WS_PQ + 2097152;
constexpr size_t WS_KVR  = WS_KVP + 1048576;
constexpr size_t WS_FLAG = WS_KVR + 32768;

#define STAMP(slot) do{}while(0)

template <int I, int N, class Fn>
__device__ __forceinline__ void static_for(Fn&& f) {
  if constexpr (I < N) { f(std::integral_constant<int, I>{}); static_for<I + 1, N>(f); }
}
template <int N> using ic = std::integral_constant<int, N>;
template <int N> __device__ __forceinline__ void wait_vm() {
  asm volatile("" ::: "memory");
  __builtin_amdgcn_s_waitcnt((N & 0xF) | (7 << 4) | (0xF << 8) | ((N >> 4) << 14));
  asm volatile("" ::: "memory");
}
__device__ __forceinline__ void bar() {
  asm volatile("" ::: "memory");
  __builtin_amdgcn_s_waitcnt(0xC07F);
  __builtin_amdgcn_s_barrier();
  asm volatile("" ::: "memory");
}

__device__ __forceinline__ float bfr(float f) { return (float)(__bf16)f; }
__device__ __forceinline__ float bf_lo(unsigned u) { return __uint_as_float(u << 16); }
__device__ __forceinline__ float bf_hi(unsigned u) { return __uint_as_float(u & 0xffff0000u); }
__device__ __forceinline__ unsigned pack2(float a, float b) {
  typedef __attribute__((ext_vector_type(2))) __bf16 bf16x2;
  bf16x2 v; v[0] = (__bf16)a; v[1] = (__bf16)b;
  return __builtin_bit_cast(unsigned, v);
}
__device__ __forceinline__ void glds16(const void* g, void* l) {
  __builtin_amdgcn_global_load_lds((gbl_cvoid_ptr)g, (lds_void_ptr)l, 16, 0, 0);
}
__device__ __forceinline__ bf16x8 tr_frag(const char* lds_a, const char* lds_b) {
  s16x4 lo = __builtin_amdgcn_ds_read_tr16_b64_v4i16((lds_s16x4_ptr)lds_a);
  s16x4 hi = __builtin_amdgcn_ds_read_tr16_b64_v4i16((lds_s16x4_ptr)lds_b);
  s16x8 v = __builtin_shufflevector(lo, hi, 0, 1, 2, 3, 4, 5, 6, 7);
  return __builtin_bit_cast(bf16x8, v);
}
__device__ __forceinline__ f32x4 mfma16(bf16x8 a, bf16x8 b, f32x4 c) {
  return __builtin_amdgcn_mfma_f32_16x16x32_bf16(a, b, c, 0, 0, 0);
}
__device__ __forceinline__ float rcp_(float v) { return __builtin_amdgcn_rcpf(v); }
__device__ __forceinline__ float exp_(float v) { return __builtin_amdgcn_exp2f(v * 1.44269504088896f); }
__device__ __forceinline__ float silu_(float v) { return v * rcp_(1.0f + exp_(-v)); }
__device__ __forceinline__ float phi_(float t) { return (t > 0.0f ? t + 1.0f : exp_(t)) + 1e-6f; }

constexpr int K1_SLOT = 24576;
constexpr int K1_XN_BYTES = 114688;
__host__ __device__ constexpr int k1_slot_off(int s) { return s < 2 ? K1_XN_BYTES + K1_SLOT * s : K1_SLOT * (s - 2); }
__host__ __device__ constexpr int k1_seg_kind(int type, int seg) {
  return type == 0 ? (seg == 0 ? 0 : 1) : (seg < 2 ? 1 : 2);
}
__host__ __device__ constexpr int k1_nstage(int type) { return type == 0 ? 14 : 21; }
__host__ __device__ constexpr int k1_cnt(int type, int t) {
  return t >= k1_nstage(type) ? 0 : (k1_seg_kind(type, t / 7) == 0 ? 3 : (k1_seg_kind(type, t / 7) == 1 ? 2 : 1));
}

template <int TYPE>
__device__ __forceinline__ void k1_body(char* smem, const __hip_bfloat16* __restrict__ x,
                                        const __hip_bfloat16* __restrict__ wqkv,
                                        const __hip_bfloat16* __restrict__ w1,
                                        const __hip_bfloat16* __restrict__ w3, char* ws, int mtile,
                                        int role) {
  const int tid = threadIdx.x, w = __builtin_amdgcn_readfirstlane(tid >> 6), l = tid & 63;
  const int rg = w & 3, cg = w >> 2;
  const int g = l >> 4, q = (l & 15) >> 2, p = l & 3;
  constexpr int NSEG = TYPE == 0 ? 2 : 3;
  constexpr int NST = k1_nstage(TYPE);
  constexpr int STAMP_K = 0; STAMP(0);

  const char* src[NSEG][3];
  const int unit0 = TYPE == 0 ? role : 8 + 2 * (role - 8);
  static_for<0, NSEG>([&](auto sc) {
    constexpr int seg = sc.value;
    constexpr int kind = k1_seg_kind(TYPE, seg);
    if constexpr (kind == 1) {
      int n0 = (TYPE == 0 ? unit0 : unit0 + seg) * 64;
      int f = (l >> 4) | ((w & 1) << 2);
      int ch = (l & 15) ^ (f << 1);
      const __hip_bfloat16* base = ch < 8 ? w1 : w3;
      for (int b = 0; b < 2; ++b) {
        int row = 8 * w + 4 * b + (l >> 4);
        src[seg][b] = (const char*)(base + (size_t)row * FF + n0 + 8 * (ch & 7));
      }
    } else if constexpr (kind == 2) {
      int n0 = 1536 + 32 * (role - 8);
      int rr = l >> 3;
      int key = ((rr >> 1) & 1) | ((w & 1) << 1);
      int ch = (l & 7) ^ (key << 1);
      const __hip_bfloat16* base = ch < 4 ? w1 : w3;
      int row = 8 * w + rr;
      src[seg][0] = (const char*)(base + (size_t)row * FF + n0 + 8 * (ch & 3));
    } else {
      for (int b = 0; b < 3; ++b) {
        int cl = 64 * (3 * w + b) + l;
        int row = cl / 24, chp = cl % 24;
        int blk = chp >> 3;
        int key = ((row >> 1) & 1) | (((row >> 3) & 1) << 1);
        int cc = (chp & 7) ^ (key << 1);
        int head = 2 * role + (cc >> 2);
        int col = blk * DM + DH * head + 8 * (cc & 3);
        if (blk == 2 && head == 15) col -= 4;
        src[seg][b] = (const char*)(wqkv + (size_t)row * (3 * DM) + col);
      }
    }
  });

  auto issue = [&](auto tc) {
    constexpr int t = tc.value;
    if constexpr (t < NST) {
      constexpr int seg = t / 7, s = t % 7, kind = k1_seg_kind(TYPE, seg);
      char* slot = smem + k1_slot_off(t % 6);
      if constexpr (kind == 1) {
        glds16(src[seg][0] + (size_t)s * 64 * FF * 2, slot + (8 * w) * 256);
        glds16(src[seg][1] + (size_t)s * 64 * FF * 2, slot + (8 * w + 4) * 256);
      } else if constexpr (kind == 2) {
        glds16(src[seg][0] + (size_t)s * 64 * FF * 2, slot + w * 1024);
      } else {
        glds16(src[seg][0] + (size_t)s * 64 * (3 * DM) * 2, slot + (3 * w) * 1024);
        glds16(src[seg][1] + (size_t)s * 64 * (3 * DM) * 2, slot + (3 * w + 1) * 1024);
        glds16(src[seg][2] + (size_t)s * 64 * (3 * DM) * 2, slot + (3 * w + 2) * 1024);
      }
    }
  };

  auto issue_piece = [&](auto tc, auto ic_) {
    constexpr int t = tc.value, i = ic_.value;
    if constexpr (t < NST && i < k1_cnt(TYPE, t)) {
      constexpr int seg = t / 7, s = t % 7, kind = k1_seg_kind(TYPE, seg);
      char* slot = smem + k1_slot_off(t % 6);
      if constexpr (kind == 1) glds16(src[seg][i] + (size_t)s * 64 * FF * 2, slot + (8 * w + 4 * i) * 256);
      else if constexpr (kind == 2) glds16(src[seg][0] + (size_t)s * 64 * FF * 2, slot + w * 1024);
      else glds16(src[seg][i] + (size_t)s * 64 * (3 * DM) * 2, slot + (3 * w + i) * 1024);
    }
  };

  bf16x8 af[2][14];
  {
    const int tile = 2 * rg + cg;
    char* reg = smem + tile * 14336;
    const char* xb = (const char*)(x + (size_t)(mtile * 128 + 16 * tile) * DM);
#pragma unroll
    for (int i = 0; i < 14; ++i) {
      const int cl = 64 * i + l, row = cl / 56, chp = cl % 56;
      glds16(xb + row * 896 + ((chp ^ (row & 7)) << 4), reg + i * 1024);
    }
    wait_vm<0>();
    issue(ic<0>{});
    issue(ic<1>{});
    const int r = l & 15;
    const unsigned ra = (unsigned)(uintptr_t)(lds_void_ptr)reg + r * 896;
    const unsigned a0 = ra + ((g ^ (r & 7)) << 4), a1 = ra + (((4 + g) ^ (r & 7)) << 4);
    u32x4 raw[14];
    asm volatile(
        "ds_read_b128 %0, %14\n\tds_read_b128 %1, %15\n\t"
        "ds_read_b128 %2, %14 offset:128\n\tds_read_b128 %3, %15 offset:128\n\t"
        "ds_read_b128 %4, %14 offset:256\n\tds_read_b128 %5, %15 offset:256\n\t"
        "ds_read_b128 %6, %14 offset:384\n\tds_read_b128 %7, %15 offset:384\n\t"
        "ds_read_b128 %8, %14 offset:512\n\tds_read_b128 %9, %15 offset:512\n\t"
        "ds_read_b128 %10, %14 offset:640\n\tds_read_b128 %11, %15 offset:640\n\t"
        "ds_read_b128 %12, %14 offset:768\n\tds_read_b128 %13, %15 offset:768\n\t"
        "s_waitcnt lgkmcnt(0)"
        : "=&v"(raw[0]), "=&v"(raw[1]), "=&v"(raw[2]), "=&v"(raw[3]), "=&v"(raw[4]), "=&v"(raw[5]), "=&v"(raw[6]),
          "=&v"(raw[7]), "=&v"(raw[8]), "=&v"(raw[9]), "=&v"(raw[10]), "=&v"(raw[11]), "=&v"(raw[12]), "=&v"(raw[13])
        : "v"(a0), "v"(a1) : "memory");
    STAMP(25);
    f32x2 d[56];
    f32x2 sv[4] = {f32x2{0.f, 0.f}, f32x2{0.f, 0.f}, f32x2{0.f, 0.f}, f32x2{0.f, 0.f}};
#pragma unroll
    for (int i = 0; i < 14; ++i)
#pragma unroll
      for (int j = 0; j < 4; ++j) {
        d[i * 4 + j] = f32x2{bf_lo(raw[i][j]), bf_hi(raw[i][j])};
        sv[j] += d[i * 4 + j];
      }
    f32x2 svs = (sv[0] + sv[1]) + (sv[2] + sv[3]);
    float sum = svs[0] + svs[1];
    sum += __shfl_xor(sum, 16); sum += __shfl_xor(sum, 32);
    const float mean = sum * (1.0f / DM);
    const f32x2 mean2 = f32x2{mean, mean};
    f32x2 vq[4] = {f32x2{0.f, 0.f}, f32x2{0.f, 0.f}, f32x2{0.f, 0.f}, f32x2{0.f, 0.f}};
#pragma unroll
    for (int i = 0; i < 56; ++i) { d[i] -= mean2; vq[i & 3] += d[i] * d[i]; }
    f32x2 vv2 = (vq[0] + vq[1]) + (vq[2] + vq[3]);
    float var = vv2[0] + vv2[1];
    var += __shfl_xor(var, 16); var += __shfl_xor(var, 32);
    var = bfr(var * (1.0f / DM));
    const float den = bfr(sqrtf(bfr(var + 1e-5f)));
    const float inv = 1.0f / den;
    const float c0 = (mean - bfr(mean)) * inv;
    const f32x2 inv2 = f32x2{inv, inv}, c02 = f32x2{c0, c0};
    bf16x8 own[14];
#pragma unroll
    for (int i = 0; i < 14; ++i) {
      u32x4 o;
#pragma unroll
      for (int j = 0; j < 4; ++j) {
        f32x2 rr = d[i * 4 + j] * inv2 + c02;
        o[j] = pack2(rr[0], rr[1]);
      }
      own[i] = __builtin_bit_cast(bf16x8, o);
      *(u32x4*)(reg + i * 1024 + l * 16) = o;
    }
    STAMP(1);
    bar();
    const char* oth = smem + (2 * rg + (1 - cg)) * 14336 + l * 16;
#pragma unroll
    for (int i = 0; i < 14; ++i) {
      af[0][i] = own[i];
      af[1][i] = *(const bf16x8*)(oth + i * 1024);
    }
  }
  wait_vm<k1_cnt(TYPE, 1)>();
  bar();
  issue(ic<2>{});
  issue(ic<3>{});
  issue(ic<4>{});
  STAMP(2);

  const int tl[2] = {mtile * 8 + rg * 2 + cg, mtile * 8 + rg * 2 + 1 - cg};
  const int f_ffn = q | ((g & 1) << 2);
  const int key2 = ((q >> 1) & 1) | ((g & 1) << 1);
  const int head = 2 * role + cg;
  int off1[4], off2[4], off0[6];
#pragma unroll
  for (int c = 0; c < 4; ++c) {
    int tile = (c >> 1) * 4 + 2 * cg + (c & 1);
    off1[c] = (8 * g + q) * 256 + ((tile ^ f_ffn) << 5) + 8 * p;
    off2[c] = (8 * g + q) * 128 + ((c ^ key2) << 5) + 8 * p;
  }
#pragma unroll
  for (int c = 0; c < 6; ++c) {
    int blk = c >> 1, ct = c & 1;
    int pi = 4 * ct + p + ((blk == 2 && head == 15) ? 1 : 0);
    pi = pi > 7 ? 7 : pi;
    int cc = 4 * cg + (pi >> 1);
    off0[c] = (8 * g + q) * 384 + blk * 128 + ((cc ^ (key2 << 1)) << 4) + 8 * (pi & 1);
  }

  bf16x8 bb[2][4];
  f32x4 acc[2][6];
  auto ldB = [&](auto tc, auto pc, bf16x8* dst) {
    constexpr int t = tc.value, ph = pc.value;
    constexpr int kind = k1_seg_kind(TYPE, t / 7);
    const char* slot = smem + k1_slot_off(t % 6);
    if constexpr (kind == 1) {
#pragma unroll
      for (int c = 0; c < 4; ++c) dst[c] = tr_frag(slot + ph * 8192 + off1[c], slot + ph * 8192 + 1024 + off1[c]);
    } else if constexpr (kind == 2) {
      if (cg == 0) {
#pragma unroll
        for (int c = 0; c < 4; ++c) dst[c] = tr_frag(slot + ph * 4096 + off2[c], slot + ph * 4096 + 512 + off2[c]);
      }
    } else {
      constexpr int ks = ph >> 1, c0 = 3 * (ph & 1);
#pragma unroll
      for (int c = 0; c < 3; ++c)
        dst[c] = tr_frag(slot + ks * 12288 + off0[c0 + c], slot + ks * 12288 + 1536 + off0[c0 + c]);
    }
  };
  auto mma = [&](auto tc, auto pc, const bf16x8* b) {
    constexpr int t = tc.value, ph = pc.value, s = t % 7;
    constexpr int kind = k1_seg_kind(TYPE, t / 7);
    const f32x4 z = f32x4{0.f, 0.f, 0.f, 0.f};
    if constexpr (kind == 1) {
      constexpr bool first = (s == 0 && ph == 0);
#pragma unroll
      for (int c = 0; c < 4; ++c) {
        acc[0][c] = mfma16(b[c], af[0][2 * s + ph], first ? z : acc[0][c]);
        acc[1][c] = mfma16(b[c], af[1][2 * s + ph], first ? z : acc[1][c]);
      }
    } else if constexpr (kind == 2) {
      constexpr bool first = (s == 0 && ph == 0);
      if (cg == 0) {
#pragma unroll
        for (int c = 0; c < 4; ++c) {
          acc[0][c] = mfma16(b[c], af[0][2 * s + ph], first ? z : acc[0][c]);
          acc[1][c] = mfma16(b[c], af[1][2 * s + ph], first ? z : acc[1][c]);
        }
      }
    } else {
      constexpr int ks = ph >> 1, c0 = 3 * (ph & 1);
      constexpr bool first = (s == 0 && ks == 0);
#pragma unroll
      for (int c = 0; c < 3; ++c) {
        if (c0 + c < 2) {
          acc[0][c0 + c] = mfma16(b[c], af[0][2 * s + ks], first ? z : acc[0][c0 + c]);
          acc[1][c0 + c] = mfma16(b[c], af[1][2 * s + ks], first ? z : acc[1][c0 + c]);
        } else {
          acc[0][c0 + c] = mfma16(af[0][2 * s + ks], b[c], first ? z : acc[0][c0 + c]);
          acc[1][c0 + c] = mfma16(af[1][2 * s + ks], b[c], first ? z : acc[1][c0 + c]);
        }
      }
    }
  };
  auto ffn_epi = [&](int kstep) {
#pragma unroll
    for (int rt = 0; rt < 2; ++rt) {
      float h[8];
#pragma unroll
      for (int c = 0; c < 2; ++c)
#pragma unroll
        for (int j = 0; j < 4; ++j) h[c * 4 + j] = silu_(acc[rt][c][j]) * acc[rt][2 + c][j];
      u32x4 o = {pack2(h[0], h[1]), pack2(h[2], h[3]), pack2(h[4], h[5]), pack2(h[6], h[7])};
      *(u32x4*)(ws + WS_H + ((size_t)(tl[rt] * 56 + kstep) * 64 + l) * 16) = o;
    }
  };

  ldB(ic<0>{}, ic<0>{}, bb[0]);

  static_for<0, NST>([&](auto tc) {
    constexpr int t = tc.value;
    constexpr int seg = t / 7, kind = k1_seg_kind(TYPE, seg);
    wait_vm<k1_cnt(TYPE, t + 2) + k1_cnt(TYPE, t + 3) + k1_cnt(TYPE, t + 4)>();
    bar(); STAMP(3 + t);
    constexpr int NPH = kind == 0 ? 4 : 2;
    static_for<0, NPH>([&](auto pc) {
      constexpr int ph = pc.value;
      if constexpr (ph > 0) __builtin_amdgcn_s_waitcnt(0xC07F);
      if constexpr (ph + 1 < NPH) ldB(tc, ic<ph + 1>{}, bb[(ph + 1) & 1]);
      else if constexpr (t + 1 < NST) ldB(ic<t + 1>{}, ic<0>{}, bb[0]);
      issue_piece(ic<t + 5>{}, pc);
      if constexpr (NPH == 2 && ph == 1) issue_piece(ic<t + 5>{}, ic<2>{});
      __builtin_amdgcn_sched_barrier(0);
      mma(tc, pc, bb[ph & 1]);
      __builtin_amdgcn_sched_barrier(0);
    });
    if constexpr (t % 7 == 6) {
      if constexpr (kind == 1) {
        ffn_epi((TYPE == 0 ? unit0 : unit0 + seg) * 2 + cg);
      } else if constexpr (kind == 2) {
        if (cg == 0) ffn_epi(48 + (role - 8));
      } else {
#pragma unroll
        for (int rt = 0; rt < 2; ++rt) {
          float v[8];
#pragma unroll
          for (int j = 0; j < 4; ++j) {
            v[j] = phi_(acc[rt][0][j]);
            float t1 = phi_(acc[rt][1][j]);
            v[4 + j] = g == 3 ? 0.f : t1;
          }
          u32x4 o = {pack2(v[0], v[1]), pack2(v[2], v[3]), pack2(v[4], v[5]), pack2(v[6], v[7])};
          *(u32x4*)(ws + WS_PQ + ((size_t)(tl[rt] * 16 + head) * 64 + l) * 16) = o;
        }
        bf16x8 pk[2], vv[2];
#pragma unroll
        for (int ct = 0; ct < 2; ++ct) {
          u32x4 a = {pack2(phi_(acc[0][2 + ct][0]), phi_(acc[0][2 + ct][1])),
                     pack2(phi_(acc[0][2 + ct][2]), phi_(acc[0][2 + ct][3])),
                     pack2(phi_(acc[1][2 + ct][0]), phi_(acc[1][2 + ct][1])),
                     pack2(phi_(acc[1][2 + ct][2]), phi_(acc[1][2 + ct][3]))};
          pk[ct] = __builtin_bit_cast(bf16x8, a);
          u32x4 b = {pack2(acc[0][4 + ct][0], acc[0][4 + ct][1]), pack2(acc[0][4 + ct][2], acc[0][4 + ct][3]),
                     pack2(acc[1][4 + ct][0], acc[1][4 + ct][1]), pack2(acc[1][4 + ct][2], acc[1][4 + ct][3])};
          if (ct == 1 && (l & 15) == 12) b = u32x4{0x3f803f80u, 0x3f803f80u, 0x3f803f80u, 0x3f803f80u};
          vv[ct] = __builtin_bit_cast(bf16x8, b);
        }
        f32x4 kvd[4];
#pragma unroll
        for (int dt = 0; dt < 2; ++dt)
#pragma unroll
          for (int et = 0; et < 2; ++et) kvd[dt * 2 + et] = mfma16(pk[dt], vv[et], f32x4{0.f, 0.f, 0.f, 0.f});
        bar();
        {
          char* sc = smem + k1_slot_off(w >> 1) + 16384 + (w & 1) * 4096;
#pragma unroll
          for (int i = 0; i < 4; ++i) *(f32x4*)(sc + i * 1024 + l * 16) = kvd[i];
        }
        bar();
        {
          const int hh = tid >> 8, idx = tid & 255;
          f32x4 sum = f32x4{0.f, 0.f, 0.f, 0.f};
#pragma unroll
          for (int r = 0; r < 4; ++r) {
            int ww = hh * 4 + r;
            sum += *(const f32x4*)(smem + k1_slot_off(ww >> 1) + 16384 + (ww & 1) * 4096 + idx * 16);
          }
          *(f32x4*)(ws + WS_KVP + ((size_t)(mtile * 16 + 2 * role + hh) * 256 + idx) * 16) = sum;
        }
      }
    }
  });
  STAMP(31);
}

__global__ __launch_bounds__(512, 2) void k1_kernel(const __hip_bfloat16* __restrict__ x,
                                                    const __hip_bfloat16* __restrict__ wqkv,
                                                    const __hip_bfloat16* __restrict__ w1,
                                                    const __hip_bfloat16* __restrict__ w3, char* ws) {
  __shared__ __attribute__((aligned(16))) char smem[163840];
  const int b = blockIdx.x;
  const int xc = b & 7, ix = b >> 3;
  const int role = (xc & 1) * 8 + (ix & 7), mtile = 4 * (xc >> 1) + (ix >> 3);
  if (b == 0 && threadIdx.x == 0) *(unsigned*)(ws + WS_FLAG) = 0u;
  if (role < 8) k1_body<0>(smem, x, wqkv, w1, w3, ws, mtile, role);
  else k1_body<1>(smem, x, wqkv, w1, w3, ws, mtile, role);
}

constexpr int K2_SLOT = 32768;
constexpr int K2_NSLOT = 4;
constexpr int K2_KV = K2_NSLOT * K2_SLOT;
constexpr int K2_NGEMM = 224;

constexpr int K2_PW = 10, K2_TF = 2;

__device__ __forceinline__ bool k2_flag_ready(unsigned lds0) {
  unsigned f;
  asm volatile("ds_read_b32 %0, %1\n\ts_waitcnt lgkmcnt(0)" : "=&v"(f) : "v"(lds0 + K2_KV + 31 * 1024) : "memory");
  return __builtin_amdgcn_readfirstlane(f) >= 16u;
}

__device__ __forceinline__ void k2_loader(char* smem, const __hip_bfloat16* __restrict__ wo,
                                          const __hip_bfloat16* __restrict__ w2, char* ws, int j, int mt, int n0) {
  const int l = threadIdx.x & 63;
  const int chs = (l & 7) ^ (((l >> 4) & 3) << 1);
  const char* src_w2 = (const char*)(w2 + (size_t)(32 * j + (l >> 3)) * DM + n0 + 8 * chs);
  const char* src_wo = (const char*)(wo + (size_t)(l >> 3) * DM + n0 + 8 * chs);
  const char* src_h = ws + WS_H + ((size_t)((mt * 4 + j) * 56) * 64 + l) * 16;
  const unsigned lds0 = (unsigned)(uintptr_t)(lds_void_ptr)smem;
  auto issue = [&](auto tc) {
    constexpr int t = tc.value;
    char* slot = smem + (t % K2_NSLOT) * K2_SLOT;
    if constexpr (t == K2_PW || t == K2_PW + 1) {
#pragma unroll
      for (int i = 0; i < 8; ++i) {
        int row0 = 224 * (t - K2_PW) + 8 * (8 * j + i);
        int row = row0 + (l >> 3);
        const char* sp = src_wo + (size_t)row0 * DM * 2;
        if (row > 447) sp -= (size_t)(row - 447) * DM * 2;
        glds16(sp, slot + (8 * j + i) * 1024);
      }
    } else if constexpr (t < 16) {
      constexpr int f = t < K2_PW ? t : t - 2;
#pragma unroll
      for (int i = 0; i < 4; ++i) glds16(src_w2 + (size_t)(f * 128 + 8 * i) * DM * 2, slot + (4 * j + i) * 1024);
#pragma unroll
      for (int i = 0; i < 4; ++i) glds16(src_h + (size_t)(f * 4 + i) * 1024, slot + 16384 + (j * 4 + i) * 1024);
    }
  };
  auto issue_kv = [&]() {
#pragma unroll
    for (int i = 0; i < 8; ++i)
      __builtin_amdgcn_global_load_lds((gbl_cvoid_ptr)(ws + WS_KVR + (size_t)((8 * j + i) * 64 + l) * 16),
                                       (lds_void_ptr)(smem + K2_KV + (8 * j + i) * 1024), 16, 0, 16);
  };
  static_for<0, K2_NSLOT - 1>([&](auto tc) { issue(tc); });
  bool kv_ready = false;
  static_for<0, 16>([&](auto tc) {
    constexpr int t = tc.value;
    wait_vm<8 * ((K2_NSLOT - 2) < (15 - t) ? (K2_NSLOT - 2) : (15 - t))>();
    bar();
    if constexpr (t == K2_PW) {
      if (!kv_ready) {
        unsigned spins = 0;
        while (__hip_atomic_load((unsigned*)(ws + WS_FLAG), __ATOMIC_RELAXED, __HIP_MEMORY_SCOPE_AGENT) < 16u) {
          __builtin_amdgcn_s_sleep(2);
          if (++spins > (1u << 24)) break;
        }
        asm volatile("" ::: "memory");
        issue_kv();
        wait_vm<0>();
        bar();
      }
    }
    issue(ic<t + K2_NSLOT - 1>{});
    if constexpr (t == K2_TF) {
      if (j == 0)
        __builtin_amdgcn_global_load_lds((gbl_cvoid_ptr)(ws + WS_FLAG), (lds_void_ptr)(smem + K2_KV + 31 * 1024), 4, 0, 16);
    }
    if constexpr (t == K2_TF + 3) kv_ready = k2_flag_ready(lds0);
    if constexpr (t == K2_TF + 4) {
      if (kv_ready) issue_kv();
    }
  });
  bar();
}

__device__ __forceinline__ void k2_gemm(char* smem, const __hip_bfloat16* __restrict__ x, char* ws,
                                        __hip_bfloat16* __restrict__ out, int w, int mt, int n0) {
  const int tid = threadIdx.x, l = tid & 63;
  const int rt = w & 3, kg = w >> 2;
  const int g = l >> 4, q = (l & 15) >> 2, p = l & 3;
  constexpr int STAMP_K = 1; STAMP(0);

  u32x4 pqf[8];
#pragma unroll
  for (int hh = 0; hh < 8; ++hh)
    pqf[hh] = *(const u32x4*)(ws + WS_PQ + ((size_t)((mt * 4 + rt) * 16 + 2 * hh + kg) * 64 + l) * 16);
  const int orow = mt * 64 + rt * 16 + (l & 15);
  u32x2 xr[2];
#pragma unroll
  for (int i = 0; i < 2; ++i) xr[i] = *(const u32x2*)(x + (size_t)orow * DM + n0 + (2 * kg + i) * 16 + 4 * g);
  asm volatile("" ::: "memory");

  f32x4 acc1[4], acc2[4];
#pragma unroll
  for (int c = 0; c < 4; ++c) { acc1[c] = f32x4{0.f, 0.f, 0.f, 0.f}; acc2[c] = acc1[c]; }

  int offa[4], offwa[4], offwb[4];
#pragma unroll
  for (int c = 0; c < 4; ++c) {
    int r1 = 4 * g + q;
    offa[c] = r1 * 128 + ((c ^ ((r1 >> 1) & 3)) << 5) + 8 * p;
    int r2 = 16 + 4 * (g > 2 ? 2 : g) + q;
    offwa[c] = (28 * kg + r1) * 128 + ((c ^ ((2 * kg + (r1 >> 1)) & 3)) << 5) + 8 * p;
    offwb[c] = (28 * kg + r2) * 128 + ((c ^ ((2 * kg + (r2 >> 1)) & 3)) << 5) + 8 * p;
  }
  bool kv_ready = false;
  const unsigned lds0 = (unsigned)(uintptr_t)(lds_void_ptr)smem;
  bf16x8 of[8];

  static_for<0, 16>([&](auto tc) {
    constexpr int t = tc.value;
    bar(); STAMP(1 + t);
    const char* slot = smem + (t % K2_NSLOT) * K2_SLOT;
    if constexpr (t == K2_PW || t == K2_PW + 1) {
      if constexpr (t == K2_PW) {
        if (!kv_ready) bar();
        f32x4 n0v[8], n1v[8];
#pragma unroll
        for (int hh = 0; hh < 8; ++hh) {
          bf16x8 pq = __builtin_bit_cast(bf16x8, pqf[hh]);
          const char* kp = smem + K2_KV + (2 * hh + kg) * 2048 + l * 16;
          n0v[hh] = mfma16(*(const bf16x8*)kp, pq, f32x4{0.f, 0.f, 0.f, 0.f});
          n1v[hh] = mfma16(*(const bf16x8*)(kp + 1024), pq, f32x4{0.f, 0.f, 0.f, 0.f});
        }
        float den[8];
#pragma unroll
        for (int hh = 0; hh < 8; ++hh) den[hh] = __shfl(n1v[hh][0], (l & 15) + 48);
#pragma unroll
        for (int hh = 0; hh < 8; ++hh) {
          const float inv = rcp_(den[hh] + 1e-6f);
          const float invm = g == 3 ? 0.f : inv;
          u32x4 ov = {pack2(n0v[hh][0] * inv, n0v[hh][1] * inv), pack2(n0v[hh][2] * inv, n0v[hh][3] * inv),
                      pack2(n1v[hh][0] * invm, n1v[hh][1] * invm), pack2(n1v[hh][2] * invm, n1v[hh][3] * invm)};
          of[hh] = __builtin_bit_cast(bf16x8, ov);
        }
      }
#pragma unroll
      for (int i = 0; i < 4; ++i) {
        const int hh = (t - K2_PW) * 4 + i;
#pragma unroll
        for (int c = 0; c < 4; ++c) {
          bf16x8 bfg = tr_frag(slot + i * 7168 + offwa[c], slot + i * 7168 + offwb[c]);
          acc2[c] = mfma16(bfg, of[hh], acc2[c]);
        }
      }
    } else {
      const bf16x8 hf0 = *(const bf16x8*)(slot + 16384 + (rt * 4 + kg) * 1024 + l * 16);
      const bf16x8 hf1 = *(const bf16x8*)(slot + 16384 + (rt * 4 + kg + 2) * 1024 + l * 16);
      bf16x8 bA[4], bB[4];
#pragma unroll
      for (int c = 0; c < 4; ++c) bA[c] = tr_frag(slot + kg * 4096 + offa[c], slot + kg * 4096 + 2048 + offa[c]);
#pragma unroll
      for (int c = 0; c < 4; ++c) bB[c] = tr_frag(slot + (kg + 2) * 4096 + offa[c], slot + (kg + 2) * 4096 + 2048 + offa[c]);
#pragma unroll
      for (int c = 0; c < 4; ++c) acc1[c] = mfma16(bA[c], hf0, acc1[c]);
#pragma unroll
      for (int c = 0; c < 4; ++c) acc1[c] = mfma16(bB[c], hf1, acc1[c]);
    }
    if constexpr (t == K2_TF + 3) kv_ready = k2_flag_ready(lds0);
  });

  STAMP(17);
  {
    char* ex = smem + K2_SLOT + w * 4096;
    *(f32x4*)(ex + 0 * 1024 + l * 16) = kg ? acc1[0] : acc1[2];
    *(f32x4*)(ex + 1 * 1024 + l * 16) = kg ? acc1[1] : acc1[3];
    *(f32x4*)(ex + 2 * 1024 + l * 16) = kg ? acc2[0] : acc2[2];
    *(f32x4*)(ex + 3 * 1024 + l * 16) = kg ? acc2[1] : acc2[3];
  }
  bar();
  {
    const char* ex = smem + K2_SLOT + (w ^ 4) * 4096;
#pragma unroll
    for (int i = 0; i < 2; ++i) {
      f32x4 a1 = (kg ? acc1[2 + i] : acc1[i]) + *(const f32x4*)(ex + i * 1024 + l * 16);
      f32x4 a2 = (kg ? acc2[2 + i] : acc2[i]) + *(const f32x4*)(ex + (2 + i) * 1024 + l * 16);
      const int col = n0 + (2 * kg + i) * 16 + 4 * g;
      float xv[4] = {bf_lo(xr[i][0]), bf_hi(xr[i][0]), bf_lo(xr[i][1]), bf_hi(xr[i][1])};
      float r[4];
#pragma unroll
      for (int jj = 0; jj < 4; ++jj) r[jj] = xv[jj] + bfr(a2[jj]) * bfr(silu_(bfr(a1[jj])));
      u32x2 ov = {pack2(r[0], r[1]), pack2(r[2], r[3])};
      *(u32x2*)(out + (size_t)orow * DM + col) = ov;
    }
  }
  STAMP(18);
}

__global__ __launch_bounds__(768, 3) void k2_kernel(const __hip_bfloat16* __restrict__ x,
                                                    const __hip_bfloat16* __restrict__ wo,
                                                    const __hip_bfloat16* __restrict__ w2,
                                                    char* ws, __hip_bfloat16* __restrict__ out) {
  __shared__ __attribute__((aligned(16))) char smem[K2_NSLOT * K2_SLOT + 32768];
  const int tid = threadIdx.x, l = tid & 63;
  const int b = blockIdx.x;
  if (b >= K2_NGEMM) {
    const int head = b - K2_NGEMM;
    if (tid < 128) {
      const int et = tid >> 6;
      f32x4 s0 = f32x4{0.f, 0.f, 0.f, 0.f}, s1 = s0;
#pragma unroll
      for (int m = 0; m < 16; ++m) {
        const char* pb = ws + WS_KVP + ((size_t)(m * 16 + head) * 256) * 16;
        s0 += *(const f32x4*)(pb + ((0 * 2 + et) * 64 + l) * 16);
        s1 += *(const f32x4*)(pb + ((1 * 2 + et) * 64 + l) * 16);
      }
      if ((l >> 4) == 3) s1 = f32x4{0.f, 0.f, 0.f, 0.f};
      if (et == 1 && (l & 15) > 12) { s0 = f32x4{0.f, 0.f, 0.f, 0.f}; s1 = s0; }
      u32x4 o = {pack2(s0[0], s0[1]), pack2(s0[2], s0[3]), pack2(s1[0], s1[1]), pack2(s1[2], s1[3])};
      const __amdgpu_buffer_rsrc_t rs = __builtin_amdgcn_make_buffer_rsrc(ws + WS_KVR, 0, 32768, 0x00020000);
      __builtin_amdgcn_raw_buffer_store_b128(o, rs, (unsigned)(((head * 2 + et) * 64 + l) * 16), 0, 16);
      asm volatile("s_waitcnt vmcnt(0)" ::: "memory");
    }
    __syncthreads();
    if (tid == 0)
      __hip_atomic_fetch_add((unsigned*)(ws + WS_FLAG), 1u, __ATOMIC_RELAXED, __HIP_MEMORY_SCOPE_AGENT);
    return;
  }
  const int xcd = b & 7, idx = b >> 3;
  const int mt = 4 * xcd + idx / 7, n0 = (idx % 7) * 64;
  const int w = __builtin_amdgcn_readfirstlane(tid >> 6);
  if (w >= 8) k2_loader(smem, wo, w2, ws, w - 8, mt, n0);
  else k2_gemm(smem, x, ws, out, w, mt, n0);
}

extern "C" void kernel_launch(void* const* d_in, const int* in_sizes, int n_in, void* d_out, int out_size,
                              void* d_ws, size_t ws_size, hipStream_t stream) {
  const __hip_bfloat16* x = (const __hip_bfloat16*)d_in[0];
  const __hip_bfloat16* wqkv = (const __hip_bfloat16*)d_in[1];
  const __hip_bfloat16* wo = (const __hip_bfloat16*)d_in[2];
  const __hip_bfloat16* w1 = (const __hip_bfloat16*)d_in[4];
  const __hip_bfloat16* w3 = (const __hip_bfloat16*)d_in[5];
  const __hip_bfloat16* w2 = (const __hip_bfloat16*)d_in[6];
  char* ws = (char*)d_ws;
  k1_kernel<<<256, 512, 0, stream>>>(x, wqkv, w1, w3, ws);
  k2_kernel<<<K2_NGEMM + 16, 768, 0, stream>>>(x, wo, w2, ws, (__hip_bfloat16*)d_out);
}
```

```cpp
#include <hip/hip_runtime.h>
#include <hip/hip_bf16.h>
#include <stdint.h>
#include <type_traits>

typedef __attribute__((ext_vector_type(8))) __bf16 bf16x8;
typedef __attribute__((ext_vector_type(4))) short s16x4;
typedef __attribute__((ext_vector_type(8))) short s16x8;
typedef __attribute__((ext_vector_type(4))) float f32x4;
typedef __attribute__((ext_vector_type(2))) float f32x2;
typedef __attribute__((ext_vector_type(4))) unsigned u32x4;
typedef __attribute__((ext_vector_type(2))) unsigned u32x2;
typedef __attribute__((address_space(3))) s16x4* lds_s16x4_ptr;
typedef __attribute__((address_space(3))) void* lds_void_ptr;
typedef const __attribute__((address_space(1))) void* gbl_cvoid_ptr;

constexpr int SEQ = 2048, DM = 448, FF = 1792, NH = 16, DH = 28;

constexpr size_t WS_H    = 0;
constexpr size_t WS_PQ   = 7340032;
constexpr size_t WS_KVP  = WS_PQ + 2097152;
constexpr size_t WS_KVR  = WS_KVP + 1048576;
constexpr size_t WS_FLAG = WS_KVR + 32768;

#define STAMP(slot) do{}while(0)

template <int I, int N, class Fn>
__device__ __forceinline__ void static_for(Fn&& f) {
  if constexpr (I < N) { f(std::integral_constant<int, I>{}); static_for<I + 1, N>(f); }
}
template <int N> using ic = std::integral_constant<int, N>;
template <int N> __device__ __forceinline__ void wait_vm() {
  asm volatile("" ::: "memory");
  __builtin_amdgcn_s_waitcnt((N & 0xF) | (7 << 4) | (0xF << 8) | ((N >> 4) << 14));
  asm volatile("" ::: "memory");
}
__device__ __forceinline__ void bar() {
  asm volatile("" ::: "memory");
  __builtin_amdgcn_s_waitcnt(0xC07F);
  __builtin_amdgcn_s_barrier();
  asm volatile("" ::: "memory");
}

__device__ __forceinline__ float bfr(float f) { return (float)(__bf16)f; }
__device__ __forceinline__ float bf_lo(unsigned u) { return __uint_as_float(u << 16); }
__device__ __forceinline__ float bf_hi(unsigned u) { return __uint_as_float(u & 0xffff0000u); }
__device__ __forceinline__ unsigned pack2(float a, float b) {
  typedef __attribute__((ext_vector_type(2))) __bf16 bf16x2;
  bf16x2 v; v[0] = (__bf16)a; v[1] = (__bf16)b;
  return __builtin_bit_cast(unsigned, v);
}
__device__ __forceinline__ void glds16(const void* g, void* l) {
  __builtin_amdgcn_global_load_lds((gbl_cvoid_ptr)g, (lds_void_ptr)l, 16, 0, 0);
}
__device__ __forceinline__ bf16x8 tr_frag(const char* lds_a, const char* lds_b) {
  s16x4 lo = __builtin_amdgcn_ds_read_tr16_b64_v4i16((lds_s16x4_ptr)lds_a);
  s16x4 hi = __builtin_amdgcn_ds_read_tr16_b64_v4i16((lds_s16x4_ptr)lds_b);
  s16x8 v = __builtin_shufflevector(lo, hi, 0, 1, 2, 3, 4, 5, 6, 7);
  return __builtin_bit_cast(bf16x8, v);
}
__device__ __forceinline__ f32x4 mfma16(bf16x8 a, bf16x8 b, f32x4 c) {
  return __builtin_amdgcn_mfma_f32_16x16x32_bf16(a, b, c, 0, 0, 0);
}
__device__ __forceinline__ float rcp_(float v) { return __builtin_amdgcn_rcpf(v); }
__device__ __forceinline__ float exp_(float v) { return __builtin_amdgcn_exp2f(v * 1.44269504088896f); }
__device__ __forceinline__ float silu_(float v) { return v * rcp_(1.0f + exp_(-v)); }
__device__ __forceinline__ float phi_(float t) { return (t > 0.0f ? t + 1.0f : exp_(t)) + 1e-6f; }

constexpr int K1_SLOT = 24576;
constexpr int K1_XN_BYTES = 114688;
__host__ __device__ constexpr int k1_slot_off(int s) { return s < 2 ? K1_XN_BYTES + K1_SLOT * s : K1_SLOT * (s - 2); }
__host__ __device__ constexpr int k1_seg_kind(int type, int seg) {
  return type == 0 ? (seg == 0 ? 0 : 1) : (seg < 2 ? 1 : 2);
}
__host__ __device__ constexpr int k1_nstage(int type) { return type == 0 ? 14 : 21; }
__host__ __device__ constexpr int k1_cnt(int type, int t) {
  return t >= k1_nstage(type) ? 0 : (k1_seg_kind(type, t / 7) == 0 ? 3 : (k1_seg_kind(type, t / 7) == 1 ? 2 : 1));
}

template <int TYPE>
__device__ __forceinline__ void k1_body(char* smem, const __hip_bfloat16* __restrict__ x,
                                        const __hip_bfloat16* __restrict__ wqkv,
                                        const __hip_bfloat16* __restrict__ w1,
                                        const __hip_bfloat16* __restrict__ w3, char* ws, int mtile,
                                        int role) {
  const int tid = threadIdx.x, w = __builtin_amdgcn_readfirstlane(tid >> 6), l = tid & 63;
  const int rg = w & 3, cg = w >> 2;
  const int g = l >> 4, q = (l & 15) >> 2, p = l & 3;
  constexpr int NSEG = TYPE == 0 ? 2 : 3;
  constexpr int NST = k1_nstage(TYPE);
  constexpr int STAMP_K = 0; STAMP(0);

  const char* src[NSEG][3];
  const int unit0 = TYPE == 0 ? role : 8 + 2 * (role - 8);
  static_for<0, NSEG>([&](auto sc) {
    constexpr int seg = sc.value;
    constexpr int kind = k1_seg_kind(TYPE, seg);
    if constexpr (kind == 1) {
      int n0 = (TYPE == 0 ? unit0 : unit0 + seg) * 64;
      int f = (l >> 4) | ((w & 1) << 2);
      int ch = (l & 15) ^ (f << 1);
      const __hip_bfloat16* base = ch < 8 ? w1 : w3;
      for (int b = 0; b < 2; ++b) {
        int row = 8 * w + 4 * b + (l >> 4);
        src[seg][b] = (const char*)(base + (size_t)row * FF + n0 + 8 * (ch & 7));
      }
    } else if constexpr (kind == 2) {
      int n0 = 1536 + 32 * (role - 8);
      int rr = l >> 3;
      int key = ((rr >> 1) & 1) | ((w & 1) << 1);
      int ch = (l & 7) ^ (key << 1);
      const __hip_bfloat16* base = ch < 4 ? w1 : w3;
      int row = 8 * w + rr;
      src[seg][0] = (const char*)(base + (size_t)row * FF + n0 + 8 * (ch & 3));
    } else {
      for (int b = 0; b < 3; ++b) {
        int cl = 64 * (3 * w + b) + l;
        int row = cl / 24, chp = cl % 24;
        int blk = chp >> 3;
        int key = ((row >> 1) & 1) | (((row >> 3) & 1) << 1);
        int cc = (chp & 7) ^ (key << 1);
        int head = 2 * role + (cc >> 2);
        int col = blk * DM + DH * head + 8 * (cc & 3);
        if (blk == 2 && head == 15) col -= 4;
        src[seg][b] = (const char*)(wqkv + (size_t)row * (3 * DM) + col);
      }
    }
  });

  auto issue = [&](auto tc) {
    constexpr int t = tc.value;
    if constexpr (t < NST) {
      constexpr int seg = t / 7, s = t % 7, kind = k1_seg_kind(TYPE, seg);
      char* slot = smem + k1_slot_off(t % 6);
      if constexpr (kind == 1) {
        glds16(src[seg][0] + (size_t)s * 64 * FF * 2, slot + (8 * w) * 256);
        glds16(src[seg][1] + (size_t)s * 64 * FF * 2, slot + (8 * w + 4) * 256);
      } else if constexpr (kind == 2) {
        glds16(src[seg][0] + (size_t)s * 64 * FF * 2, slot + w * 1024);
      } else {
        glds16(src[seg][0] + (size_t)s * 64 * (3 * DM) * 2, slot + (3 * w) * 1024);
        glds16(src[seg][1] + (size_t)s * 64 * (3 * DM) * 2, slot + (3 * w + 1) * 1024);
        glds16(src[seg][2] + (size_t)s * 64 * (3 * DM) * 2, slot + (3 * w + 2) * 1024);
      }
    }
  };

  auto issue_piece = [&](auto tc, auto ic_) {
    constexpr int t = tc.value, i = ic_.value;
    if constexpr (t < NST && i < k1_cnt(TYPE, t)) {
      constexpr int seg = t / 7, s = t % 7, kind = k1_seg_kind(TYPE, seg);
      char* slot = smem + k1_slot_off(t % 6);
      if constexpr (kind == 1) glds16(src[seg][i] + (size_t)s * 64 * FF * 2, slot + (8 * w + 4 * i) * 256);
      else if constexpr (kind == 2) glds16(src[seg][0] + (size_t)s * 64 * FF * 2, slot + w * 1024);
      else glds16(src[seg][i] + (size_t)s * 64 * (3 * DM) * 2, slot + (3 * w + i) * 1024);
    }
  };

  bf16x8 af[2][14];
  {
    const int tile = 2 * rg + cg;
    char* reg = smem + tile * 14336;
    const char* xb = (const char*)(x + (size_t)(mtile * 128 + 16 * tile) * DM);
#pragma unroll
    for (int i = 0; i < 14; ++i) {
      const int cl = 64 * i + l, row = cl / 56, chp = cl % 56;
      glds16(xb + row * 896 + ((chp ^ (row & 7)) << 4), reg + i * 1024);
    }
    wait_vm<0>();
    issue(ic<0>{});
    issue(ic<1>{});
    const int r = l & 15;
    const unsigned ra = (unsigned)(uintptr_t)(lds_void_ptr)reg + r * 896;
    const unsigned a0 = ra + ((g ^ (r & 7)) << 4), a1 = ra + (((4 + g) ^ (r & 7)) << 4);
    u32x4 raw[14];
    asm volatile(
        "ds_read_b128 %0, %14\n\tds_read_b128 %1, %15\n\t"
        "ds_read_b128 %2, %14 offset:128\n\tds_read_b128 %3, %15 offset:128\n\t"
        "ds_read_b128 %4, %14 offset:256\n\tds_read_b128 %5, %15 offset:256\n\t"
        "ds_read_b128 %6, %14 offset:384\n\tds_read_b128 %7, %15 offset:384\n\t"
        "ds_read_b128 %8, %14 offset:512\n\tds_read_b128 %9, %15 offset:512\n\t"
        "ds_read_b128 %10, %14 offset:640\n\tds_read_b128 %11, %15 offset:640\n\t"
        "ds_read_b128 %12, %14 offset:768\n\tds_read_b128 %13, %15 offset:768\n\t"
        "s_waitcnt lgkmcnt(0)"
        : "=&v"(raw[0]), "=&v"(raw[1]), "=&v"(raw[2]), "=&v"(raw[3]), "=&v"(raw[4]), "=&v"(raw[5]), "=&v"(raw[6]),
          "=&v"(raw[7]), "=&v"(raw[8]), "=&v"(raw[9]), "=&v"(raw[10]), "=&v"(raw[11]), "=&v"(raw[12]), "=&v"(raw[13])
        : "v"(a0), "v"(a1) : "memory");
    STAMP(25);
    f32x2 d[56];
    f32x2 sv[4] = {f32x2{0.f, 0.f}, f32x2{0.f, 0.f}, f32x2{0.f, 0.f}, f32x2{0.f, 0.f}};
#pragma unroll
    for (int i = 0; i < 14; ++i)
#pragma unroll
      for (int j = 0; j < 4; ++j) {
        d[i * 4 + j] = f32x2{bf_lo(raw[i][j]), bf_hi(raw[i][j])};
        sv[j] += d[i * 4 + j];
      }
    f32x2 svs = (sv[0] + sv[1]) + (sv[2] + sv[3]);
    float sum = svs[0] + svs[1];
    sum += __shfl_xor(sum, 16); sum += __shfl_xor(sum, 32);
    const float mean = sum * (1.0f / DM);
    const f32x2 mean2 = f32x2{mean, mean};
    f32x2 vq[4] = {f32x2{0.f, 0.f}, f32x2{0.f, 0.f}, f32x2{0.f, 0.f}, f32x2{0.f, 0.f}};
#pragma unroll
    for (int i = 0; i < 56; ++i) { d[i] -= mean2; vq[i & 3] += d[i] * d[i]; }
    f32x2 vv2 = (vq[0] + vq[1]) + (vq[2] + vq[3]);
    float var = vv2[0] + vv2[1];
    var += __shfl_xor(var, 16); var += __shfl_xor(var, 32);
    var = bfr(var * (1.0f / DM));
    const float den = bfr(sqrtf(bfr(var + 1e-5f)));
    const float inv = 1.0f / den;
    const float c0 = (mean - bfr(mean)) * inv;
    const f32x2 inv2 = f32x2{inv, inv}, c02 = f32x2{c0, c0};
    bf16x8 own[14];
#pragma unroll
    for (int i = 0; i < 14; ++i) {
      u32x4 o;
#pragma unroll
      for (int j = 0; j < 4; ++j) {
        f32x2 rr = d[i * 4 + j] * inv2 + c02;
        o[j] = pack2(rr[0], rr[1]);
      }
      own[i] = __builtin_bit_cast(bf16x8, o);
      *(u32x4*)(reg + i * 1024 + l * 16) = o;
    }
    STAMP(1);
    bar();
    const char* oth = smem + (2 * rg + (1 - cg)) * 14336 + l * 16;
#pragma unroll
    for (int i = 0; i < 14; ++i) {
      af[0][i] = own[i];
      af[1][i] = *(const bf16x8*)(oth + i * 1024);
    }
  }
  wait_vm<k1_cnt(TYPE, 1)>();
  bar();
  issue(ic<2>{});
  issue(ic<3>{});
  STAMP(2);

  const int tl[2] = {mtile * 8 + rg * 2 + cg, mtile * 8 + rg * 2 + 1 - cg};
  const int f_ffn = q | ((g & 1) << 2);
  const int key2 = ((q >> 1) & 1) | ((g & 1) << 1);
  const int head = 2 * role + cg;
  int off1[4], off2[4], off0[6];
#pragma unroll
  for (int c = 0; c < 4; ++c) {
    int tile = (c >> 1) * 4 + 2 * cg + (c & 1);
    off1[c] = (8 * g + q) * 256 + ((tile ^ f_ffn) << 5) + 8 * p;
    off2[c] = (8 * g + q) * 128 + ((c ^ key2) << 5) + 8 * p;
  }
#pragma unroll
  for (int c = 0; c < 6; ++c) {
    int blk = c >> 1, ct = c & 1;
    int pi = 4 * ct + p + ((blk == 2 && head == 15) ? 1 : 0);
    pi = pi > 7 ? 7 : pi;
    int cc = 4 * cg + (pi >> 1);
    off0[c] = (8 * g + q) * 384 + blk * 128 + ((cc ^ (key2 << 1)) << 4) + 8 * (pi & 1);
  }

  bf16x8 bb[2][4];
  f32x4 acc[2][6];
  auto ldB = [&](auto tc, auto pc, bf16x8* dst) {
    constexpr int t = tc.value, ph = pc.value;
    constexpr int kind = k1_seg_kind(TYPE, t / 7);
    const char* slot = smem + k1_slot_off(t % 6);
    if constexpr (kind == 1) {
#pragma unroll
      for (int c = 0; c < 4; ++c) dst[c] = tr_frag(slot + ph * 8192 + off1[c], slot + ph * 8192 + 1024 + off1[c]);
    } else if constexpr (kind == 2) {
      if (cg == 0) {
#pragma unroll
        for (int c = 0; c < 4; ++c) dst[c] = tr_frag(slot + ph * 4096 + off2[c], slot + ph * 4096 + 512 + off2[c]);
      }
    } else {
      constexpr int ks = ph >> 1, c0 = 3 * (ph & 1);
#pragma unroll
      for (int c = 0; c < 3; ++c)
        dst[c] = tr_frag(slot + ks * 12288 + off0[c0 + c], slot + ks * 12288 + 1536 + off0[c0 + c]);
    }
  };
  auto mma = [&](auto tc, auto pc, const bf16x8* b) {
    constexpr int t = tc.value, ph = pc.value, s = t % 7;
    constexpr int kind = k1_seg_kind(TYPE, t / 7);
    const f32x4 z = f32x4{0.f, 0.f, 0.f, 0.f};
    if constexpr (kind == 1) {
      constexpr bool first = (s == 0 && ph == 0);
#pragma unroll
      for (int c = 0; c < 4; ++c) {
        acc[0][c] = mfma16(b[c], af[0][2 * s + ph], first ? z : acc[0][c]);
        acc[1][c] = mfma16(b[c], af[1][2 * s + ph], first ? z : acc[1][c]);
      }
    } else if constexpr (kind == 2) {
      constexpr bool first = (s == 0 && ph == 0);
      if (cg == 0) {
#pragma unroll
        for (int c = 0; c < 4; ++c) {
          acc[0][c] = mfma16(b[c], af[0][2 * s + ph], first ? z : acc[0][c]);
          acc[1][c] = mfma16(b[c], af[1][2 * s + ph], first ? z : acc[1][c]);
        }
      }
    } else {
      constexpr int ks = ph >> 1, c0 = 3 * (ph & 1);
      constexpr bool first = (s == 0 && ks == 0);
#pragma unroll
      for (int c = 0; c < 3; ++c) {
        if (c0 + c < 2) {
          acc[0][c0 + c] = mfma16(b[c], af[0][2 * s + ks], first ? z : acc[0][c0 + c]);
          acc[1][c0 + c] = mfma16(b[c], af[1][2 * s + ks], first ? z : acc[1][c0 + c]);
        } else {
          acc[0][c0 + c] = mfma16(af[0][2 * s + ks], b[c], first ? z : acc[0][c0 + c]);
          acc[1][c0 + c] = mfma16(af[1][2 * s + ks], b[c], first ? z : acc[1][c0 + c]);
        }
      }
    }
  };
  auto ffn_epi = [&](int kstep) {
#pragma unroll
    for (int rt = 0; rt < 2; ++rt) {
      float h[8];
#pragma unroll
      for (int c = 0; c < 2; ++c)
#pragma unroll
        for (int j = 0; j < 4; ++j) h[c * 4 + j] = silu_(acc[rt][c][j]) * acc[rt][2 + c][j];
      u32x4 o = {pack2(h[0], h[1]), pack2(h[2], h[3]), pack2(h[4], h[5]), pack2(h[6], h[7])};
      *(u32x4*)(ws + WS_H + ((size_t)(tl[rt] * 56 + kstep) * 64 + l) * 16) = o;
    }
  };

  ldB(ic<0>{}, ic<0>{}, bb[0]);

  static_for<0, NST>([&](auto tc) {
    constexpr int t = tc.value;
    constexpr int seg = t / 7, kind = k1_seg_kind(TYPE, seg);
    if constexpr (t % 2 == 0) {
      wait_vm<k1_cnt(TYPE, t + 3)>();
      bar();
    }
    STAMP(3 + t);
    constexpr int NPH = kind == 0 ? 4 : 2;
    static_for<0, NPH>([&](auto pc) {
      constexpr int ph = pc.value;
      if constexpr (ph > 0 || t % 2 == 1) __builtin_amdgcn_s_waitcnt(0xC07F);
      if constexpr (ph + 1 < NPH) ldB(tc, ic<ph + 1>{}, bb[(ph + 1) & 1]);
      else if constexpr (t + 1 < NST) ldB(ic<t + 1>{}, ic<0>{}, bb[0]);
      issue_piece(ic<t + 4>{}, pc);
      if constexpr (NPH == 2 && ph == 1) issue_piece(ic<t + 4>{}, ic<2>{});
      __builtin_amdgcn_sched_barrier(0);
      mma(tc, pc, bb[ph & 1]);
      __builtin_amdgcn_sched_barrier(0);
    });
    if constexpr (t % 7 == 6) {
      if constexpr (kind == 1) {
        ffn_epi((TYPE == 0 ? unit0 : unit0 + seg) * 2 + cg);
      } else if constexpr (kind == 2) {
        if (cg == 0) ffn_epi(48 + (role - 8));
      } else {
#pragma unroll
        for (int rt = 0; rt < 2; ++rt) {
          float v[8];
#pragma unroll
          for (int j = 0; j < 4; ++j) {
            v[j] = phi_(acc[rt][0][j]);
            float t1 = phi_(acc[rt][1][j]);
            v[4 + j] = g == 3 ? 0.f : t1;
          }
          u32x4 o = {pack2(v[0], v[1]), pack2(v[2], v[3]), pack2(v[4], v[5]), pack2(v[6], v[7])};
          *(u32x4*)(ws + WS_PQ + ((size_t)(tl[rt] * 16 + head) * 64 + l) * 16) = o;
        }
        bf16x8 pk[2], vv[2];
#pragma unroll
        for (int ct = 0; ct < 2; ++ct) {
          u32x4 a = {pack2(phi_(acc[0][2 + ct][0]), phi_(acc[0][2 + ct][1])),
                     pack2(phi_(acc[0][2 + ct][2]), phi_(acc[0][2 + ct][3])),
                     pack2(phi_(acc[1][2 + ct][0]), phi_(acc[1][2 + ct][1])),
                     pack2(phi_(acc[1][2 + ct][2]), phi_(acc[1][2 + ct][3]))};
          pk[ct] = __builtin_bit_cast(bf16x8, a);
          u32x4 b = {pack2(acc[0][4 + ct][0], acc[0][4 + ct][1]), pack2(acc[0][4 + ct][2], acc[0][4 + ct][3]),
                     pack2(acc[1][4 + ct][0], acc[1][4 + ct][1]), pack2(acc[1][4 + ct][2], acc[1][4 + ct][3])};
          if (ct == 1 && (l & 15) == 12) b = u32x4{0x3f803f80u, 0x3f803f80u, 0x3f803f80u, 0x3f803f80u};
          vv[ct] = __builtin_bit_cast(bf16x8, b);
        }
        f32x4 kvd[4];
#pragma unroll
        for (int dt = 0; dt < 2; ++dt)
#pragma unroll
          for (int et = 0; et < 2; ++et) kvd[dt * 2 + et] = mfma16(pk[dt], vv[et], f32x4{0.f, 0.f, 0.f, 0.f});
        bar();
        {
          char* sc = smem + k1_slot_off(w >> 1) + 16384 + (w & 1) * 4096;
#pragma unroll
          for (int i = 0; i < 4; ++i) *(f32x4*)(sc + i * 1024 + l * 16) = kvd[i];
        }
        bar();
        {
          const int hh = tid >> 8, idx = tid & 255;
          f32x4 sum = f32x4{0.f, 0.f, 0.f, 0.f};
#pragma unroll
          for (int r = 0; r < 4; ++r) {
            int ww = hh * 4 + r;
            sum += *(const f32x4*)(smem + k1_slot_off(ww >> 1) + 16384 + (ww & 1) * 4096 + idx * 16);
          }
          *(f32x4*)(ws + WS_KVP + ((size_t)(mtile * 16 + 2 * role + hh) * 256 + idx) * 16) = sum;
        }
      }
    }
  });
  STAMP(31);
}

__global__ __launch_bounds__(512, 2) void k1_kernel(const __hip_bfloat16* __restrict__ x,
                                                    const __hip_bfloat16* __restrict__ wqkv,
                                                    const __hip_bfloat16* __restrict__ w1,
                                                    const __hip_bfloat16* __restrict__ w3, char* ws) {
  __shared__ __attribute__((aligned(16))) char smem[163840];
  const int b = blockIdx.x;
  const int xc = b & 7, ix = b >> 3;
  const int role = (xc & 1) * 8 + (ix & 7), mtile = 4 * (xc >> 1) + (ix >> 3);
  if (b == 0 && threadIdx.x == 0) *(unsigned*)(ws + WS_FLAG) = 0u;
  if (role < 8) k1_body<0>(smem, x, wqkv, w1, w3, ws, mtile, role);
  else k1_body<1>(smem, x, wqkv, w1, w3, ws, mtile, role);
}

constexpr int K2_SLOT = 32768;
constexpr int K2_NSLOT = 4;
constexpr int K2_KV = K2_NSLOT * K2_SLOT;
constexpr int K2_NGEMM = 224;

constexpr int K2_PW = 10, K2_TF = 2;

__device__ __forceinline__ bool k2_flag_ready(unsigned lds0) {
  unsigned f;
  asm volatile("ds_read_b32 %0, %1\n\ts_waitcnt lgkmcnt(0)" : "=&v"(f) : "v"(lds0 + K2_KV + 31 * 1024) : "memory");
  return __builtin_amdgcn_readfirstlane(f) >= 16u;
}

__device__ __forceinline__ void k2_loader(char* smem, const __hip_bfloat16* __restrict__ wo,
                                          const __hip_bfloat16* __restrict__ w2, char* ws, int j, int mt, int n0) {
  const int l = threadIdx.x & 63;
  const int chs = (l & 7) ^ (((l >> 4) & 3) << 1);
  const char* src_w2 = (const char*)(w2 + (size_t)(32 * j + (l >> 3)) * DM + n0 + 8 * chs);
  const char* src_wo = (const char*)(wo + (size_t)(l >> 3) * DM + n0 + 8 * chs);
  const char* src_h = ws + WS_H + ((size_t)((mt * 4 + j) * 56) * 64 + l) * 16;
  const unsigned lds0 = (unsigned)(uintptr_t)(lds_void_ptr)smem;
  auto issue = [&](auto tc) {
    constexpr int t = tc.value;
    char* slot = smem + (t % K2_NSLOT) * K2_SLOT;
    if constexpr (t == K2_PW || t == K2_PW + 1) {
#pragma unroll
      for (int i = 0; i < 8; ++i) {
        int row0 = 224 * (t - K2_PW) + 8 * (8 * j + i);
        int row = row0 + (l >> 3);
        const char* sp = src_wo + (size_t)row0 * DM * 2;
        if (row > 447) sp -= (size_t)(row - 447) * DM * 2;
        glds16(sp, slot + (8 * j + i) * 1024);
      }
    } else if constexpr (t < 16) {
      constexpr int f = t < K2_PW ? t : t - 2;
#pragma unroll
      for (int i = 0; i < 4; ++i) glds16(src_w2 + (size_t)(f * 128 + 8 * i) * DM * 2, slot + (4 * j + i) * 1024);
#pragma unroll
      for (int i = 0; i < 4; ++i) glds16(src_h + (size_t)(f * 4 + i) * 1024, slot + 16384 + (j * 4 + i) * 1024);
    }
  };
  auto issue_kv = [&]() {
#pragma unroll
    for (int i = 0; i < 8; ++i)
      __builtin_amdgcn_global_load_lds((gbl_cvoid_ptr)(ws + WS_KVR + (size_t)((8 * j + i) * 64 + l) * 16),
                                       (lds_void_ptr)(smem + K2_KV + (8 * j + i) * 1024), 16, 0, 16);
  };
  static_for<0, K2_NSLOT - 1>([&](auto tc) { issue(tc); });
  bool kv_ready = false;
  static_for<0, 16>([&](auto tc) {
    constexpr int t = tc.value;
    wait_vm<8 * ((K2_NSLOT - 2) < (15 - t) ? (K2_NSLOT - 2) : (15 - t))>();
    bar();
    if constexpr (t == K2_PW) {
      if (!kv_ready) {
        unsigned spins = 0;
        while (__hip_atomic_load((unsigned*)(ws + WS_FLAG), __ATOMIC_RELAXED, __HIP_MEMORY_SCOPE_AGENT) < 16u) {
          __builtin_amdgcn_s_sleep(2);
          if (++spins > (1u << 24)) break;
        }
        asm volatile("" ::: "memory");
        issue_kv();
        wait_vm<0>();
        bar();
      }
    }
    issue(ic<t + K2_NSLOT - 1>{});
    if constexpr (t == K2_TF) {
      if (j == 0)
        __builtin_amdgcn_global_load_lds((gbl_cvoid_ptr)(ws + WS_FLAG), (lds_void_ptr)(smem + K2_KV + 31 * 1024), 4, 0, 16);
    }
    if constexpr (t == K2_TF + 3) kv_ready = k2_flag_ready(lds0);
    if constexpr (t == K2_TF + 4) {
      if (kv_ready) issue_kv();
    }
  });
  bar();
}

__device__ __forceinline__ void k2_gemm(char* smem, const __hip_bfloat16* __restrict__ x, char* ws,
                                        __hip_bfloat16* __restrict__ out, int w, int mt, int n0) {
  const int tid = threadIdx.x, l = tid & 63;
  const int rt = w & 3, kg = w >> 2;
  const int g = l >> 4, q = (l & 15) >> 2, p = l & 3;
  constexpr int STAMP_K = 1; STAMP(0);

  u32x4 pqf[8];
#pragma unroll
  for (int hh = 0; hh < 8; ++hh)
    pqf[hh] = *(const u32x4*)(ws + WS_PQ + ((size_t)((mt * 4 + rt) * 16 + 2 * hh + kg) * 64 + l) * 16);
  const int orow = mt * 64 + rt * 16 + (l & 15);
  u32x2 xr[2];
#pragma unroll
  for (int i = 0; i < 2; ++i) xr[i] = *(const u32x2*)(x + (size_t)orow * DM + n0 + (2 * kg + i) * 16 + 4 * g);
  asm volatile("" ::: "memory");

  f32x4 acc1[4], acc2[4];
#pragma unroll
  for (int c = 0; c < 4; ++c) { acc1[c] = f32x4{0.f, 0.f, 0.f, 0.f}; acc2[c] = acc1[c]; }

  int offa[4], offwa[4], offwb[4];
#pragma unroll
  for (int c = 0; c < 4; ++c) {
    int r1 = 4 * g + q;
    offa[c] = r1 * 128 + ((c ^ ((r1 >> 1) & 3)) << 5) + 8 * p;
    int r2 = 16 + 4 * (g > 2 ? 2 : g) + q;
    offwa[c] = (28 * kg + r1) * 128 + ((c ^ ((2 * kg + (r1 >> 1)) & 3)) << 5) + 8 * p;
    offwb[c] = (28 * kg + r2) * 128 + ((c ^ ((2 * kg + (r2 >> 1)) & 3)) << 5) + 8 * p;
  }
  bool kv_ready = false;
  const unsigned lds0 = (unsigned)(uintptr_t)(lds_void_ptr)smem;
  bf16x8 of[8];

  static_for<0, 16>([&](auto tc) {
    constexpr int t = tc.value;
    bar(); STAMP(1 + t);
    const char* slot = smem + (t % K2_NSLOT) * K2_SLOT;
    if constexpr (t == K2_PW || t == K2_PW + 1) {
      if constexpr (t == K2_PW) {
        if (!kv_ready) bar();
        f32x4 n0v[8], n1v[8];
#pragma unroll
        for (int hh = 0; hh < 8; ++hh) {
          bf16x8 pq = __builtin_bit_cast(bf16x8, pqf[hh]);
          const char* kp = smem + K2_KV + (2 * hh + kg) * 2048 + l * 16;
          n0v[hh] = mfma16(*(const bf16x8*)kp, pq, f32x4{0.f, 0.f, 0.f, 0.f});
          n1v[hh] = mfma16(*(const bf16x8*)(kp + 1024), pq, f32x4{0.f, 0.f, 0.f, 0.f});
        }
        float den[8];
#pragma unroll
        for (int hh = 0; hh < 8; ++hh) den[hh] = __shfl(n1v[hh][0], (l & 15) + 48);
#pragma unroll
        for (int hh = 0; hh < 8; ++hh) {
          const float inv = rcp_(den[hh] + 1e-6f);
          const float invm = g == 3 ? 0.f : inv;
          u32x4 ov = {pack2(n0v[hh][0] * inv, n0v[hh][1] * inv), pack2(n0v[hh][2] * inv, n0v[hh][3] * inv),
                      pack2(n1v[hh][0] * invm, n1v[hh][1] * invm), pack2(n1v[hh][2] * invm, n1v[hh][3] * invm)};
          of[hh] = __builtin_bit_cast(bf16x8, ov);
        }
      }
#pragma unroll
      for (int i = 0; i < 4; ++i) {
        const int hh = (t - K2_PW) * 4 + i;
#pragma unroll
        for (int c = 0; c < 4; ++c) {
          bf16x8 bfg = tr_frag(slot + i * 7168 + offwa[c], slot + i * 7168 + offwb[c]);
          acc2[c] = mfma16(bfg, of[hh], acc2[c]);
        }
      }
    } else {
      const bf16x8 hf0 = *(const bf16x8*)(slot + 16384 + (rt * 4 + kg) * 1024 + l * 16);
      const bf16x8 hf1 = *(const bf16x8*)(slot + 16384 + (rt * 4 + kg + 2) * 1024 + l * 16);
      bf16x8 bA[4], bB[4];
#pragma unroll
      for (int c = 0; c < 4; ++c) bA[c] = tr_frag(slot + kg * 4096 + offa[c], slot + kg * 4096 + 2048 + offa[c]);
#pragma unroll
      for (int c = 0; c < 4; ++c) bB[c] = tr_frag(slot + (kg + 2) * 4096 + offa[c], slot + (kg + 2) * 4096 + 2048 + offa[c]);
#pragma unroll
      for (int c = 0; c < 4; ++c) acc1[c] = mfma16(bA[c], hf0, acc1[c]);
#pragma unroll
      for (int c = 0; c < 4; ++c) acc1[c] = mfma16(bB[c], hf1, acc1[c]);
    }
    if constexpr (t == K2_TF + 3) kv_ready = k2_flag_ready(lds0);
  });

  STAMP(17);
  {
    char* ex = smem + K2_SLOT + w * 4096;
    *(f32x4*)(ex + 0 * 1024 + l * 16) = kg ? acc1[0] : acc1[2];
    *(f32x4*)(ex + 1 * 1024 + l * 16) = kg ? acc1[1] : acc1[3];
    *(f32x4*)(ex + 2 * 1024 + l * 16) = kg ? acc2[0] : acc2[2];
    *(f32x4*)(ex + 3 * 1024 + l * 16) = kg ? acc2[1] : acc2[3];
  }
  bar();
  {
    const char* ex = smem + K2_SLOT + (w ^ 4) * 4096;
#pragma unroll
    for (int i = 0; i < 2; ++i) {
      f32x4 a1 = (kg ? acc1[2 + i] : acc1[i]) + *(const f32x4*)(ex + i * 1024 + l * 16);
      f32x4 a2 = (kg ? acc2[2 + i] : acc2[i]) + *(const f32x4*)(ex + (2 + i) * 1024 + l * 16);
      const int col = n0 + (2 * kg + i) * 16 + 4 * g;
      float xv[4] = {bf_lo(xr[i][0]), bf_hi(xr[i][0]), bf_lo(xr[i][1]), bf_hi(xr[i][1])};
      float r[4];
#pragma unroll
      for (int jj = 0; jj < 4; ++jj) r[jj] = xv[jj] + bfr(a2[jj]) * bfr(silu_(bfr(a1[jj])));
      u32x2 ov = {pack2(r[0], r[1]), pack2(r[2], r[3])};
      *(u32x2*)(out + (size_t)orow * DM + col) = ov;
    }
  }
  STAMP(18);
}

__global__ __launch_bounds__(768, 3) void k2_kernel(const __hip_bfloat16* __restrict__ x,
                                                    const __hip_bfloat16* __restrict__ wo,
                                                    const __hip_bfloat16* __restrict__ w2,
                                                    char* ws, __hip_bfloat16* __restrict__ out) {
  __shared__ __attribute__((aligned(16))) char smem[K2_NSLOT * K2_SLOT + 32768];
  const int tid = threadIdx.x, l = tid & 63;
  const int b = blockIdx.x;
  if (b >= K2_NGEMM) {
    const int head = b - K2_NGEMM;
    if (tid < 128) {
      const int et = tid >> 6;
      f32x4 s0 = f32x4{0.f, 0.f, 0.f, 0.f}, s1 = s0;
#pragma unroll
      for (int m = 0; m < 16; ++m) {
        const char* pb = ws + WS_KVP + ((size_t)(m * 16 + head) * 256) * 16;
        s0 += *(const f32x4*)(pb + ((0 * 2 + et) * 64 + l) * 16);
        s1 += *(const f32x4*)(pb + ((1 * 2 + et) * 64 + l) * 16);
      }
      if ((l >> 4) == 3) s1 = f32x4{0.f, 0.f, 0.f, 0.f};
      if (et == 1 && (l & 15) > 12) { s0 = f32x4{0.f, 0.f, 0.f, 0.f}; s1 = s0; }
      u32x4 o = {pack2(s0[0], s0[1]), pack2(s0[2], s0[3]), pack2(s1[0], s1[1]), pack2(s1[2], s1[3])};
      const __amdgpu_buffer_rsrc_t rs = __builtin_amdgcn_make_buffer_rsrc(ws + WS_KVR, 0, 32768, 0x00020000);
      __builtin_amdgcn_raw_buffer_store_b128(o, rs, (unsigned)(((head * 2 + et) * 64 + l) * 16), 0, 16);
      asm volatile("s_waitcnt vmcnt(0)" ::: "memory");
    }
    __syncthreads();
    if (tid == 0)
      __hip_atomic_fetch_add((unsigned*)(ws + WS_FLAG), 1u, __ATOMIC_RELAXED, __HIP_MEMORY_SCOPE_AGENT);
    return;
  }
  const int xcd = b & 7, idx = b >> 3;
  const int mt = 4 * xcd + idx / 7, n0 = (idx % 7) * 64;
  const int w = __builtin_amdgcn_readfirstlane(tid >> 6);
  if (w >= 8) k2_loader(smem, wo, w2, ws, w - 8, mt, n0);
  else k2_gemm(smem, x, ws, out, w, mt, n0);
}

extern "C" void kernel_launch(void* const* d_in, const int* in_sizes, int n_in, void* d_out, int out_size,
                              void* d_ws, size_t ws_size, hipStream_t stream) {
  const __hip_bfloat16* x = (const __hip_bfloat16*)d_in[0];
  const __hip_bfloat16* wqkv = (const __hip_bfloat16*)d_in[1];
  const __hip_bfloat16* wo = (const __hip_bfloat16*)d_in[2];
  const __hip_bfloat16* w1 = (const __hip_bfloat16*)d_in[4];
  const __hip_bfloat16* w3 = (const __hip_bfloat16*)d_in[5];
  const __hip_bfloat16* w2 = (const __hip_bfloat16*)d_in[6];
  char* ws = (char*)d_ws;
  k1_kernel<<<256, 512, 0, stream>>>(x, wqkv, w1, w3, ws);
  k2_kernel<<<K2_NGEMM + 16, 768, 0, stream>>>(x, wo, w2, ws, (__hip_bfloat16*)d_out);
}
```

```cpp
#include <hip/hip_runtime.h>
#include <hip/hip_bf16.h>
#include <stdint.h>
#include <type_traits>

typedef __attribute__((ext_vector_type(8))) __bf16 bf16x8;
typedef __attribute__((ext_vector_type(4))) short s16x4;
typedef __attribute__((ext_vector_type(8))) short s16x8;
typedef __attribute__((ext_vector_type(4))) float f32x4;
typedef __attribute__((ext_vector_type(2))) float f32x2;
typedef __attribute__((ext_vector_type(4))) unsigned u32x4;
typedef __attribute__((ext_vector_type(2))) unsigned u32x2;
typedef __attribute__((address_space(3))) s16x4* lds_s16x4_ptr;
typedef __attribute__((address_space(3))) void* lds_void_ptr;
typedef const __attribute__((address_space(1))) void* gbl_cvoid_ptr;

constexpr int SEQ = 2048, DM = 448, FF = 1792, NH = 16, DH = 28;

constexpr size_t WS_H    = 0;
constexpr size_t WS_PQ   = 7340032;
constexpr size_t WS_KVP  = WS_PQ + 2097152;
constexpr size_t WS_KVR  = WS_KVP + 1048576;
constexpr size_t WS_FLAG = WS_KVR + 32768;

#define STAMP(slot) do{}while(0)

template <int I, int N, class Fn>
__device__ __forceinline__ void static_for(Fn&& f) {
  if constexpr (I < N) { f(std::integral_constant<int, I>{}); static_for<I + 1, N>(f); }
}
template <int N> using ic = std::integral_constant<int, N>;
template <int N> __device__ __forceinline__ void wait_vm() {
  asm volatile("" ::: "memory");
  __builtin_amdgcn_s_waitcnt((N & 0xF) | (7 << 4) | (0xF << 8) | ((N >> 4) << 14));
  asm volatile("" ::: "memory");
}
__device__ __forceinline__ void bar() {
  asm volatile("" ::: "memory");
  __builtin_amdgcn_s_waitcnt(0xC07F);
  __builtin_amdgcn_s_barrier();
  asm volatile("" ::: "memory");
}

__device__ __forceinline__ float bfr(float f) { return (float)(__bf16)f; }
__device__ __forceinline__ float bf_lo(unsigned u) { return __uint_as_float(u << 16); }
__device__ __forceinline__ float bf_hi(unsigned u) { return __uint_as_float(u & 0xffff0000u); }
__device__ __forceinline__ unsigned pack2(float a, float b) {
  typedef __attribute__((ext_vector_type(2))) __bf16 bf16x2;
  bf16x2 v; v[0] = (__bf16)a; v[1] = (__bf16)b;
  return __builtin_bit_cast(unsigned, v);
}
__device__ __forceinline__ void glds16(const void* g, void* l) {
  __builtin_amdgcn_global_load_lds((gbl_cvoid_ptr)g, (lds_void_ptr)l, 16, 0, 0);
}
__device__ __forceinline__ bf16x8 tr_frag(const char* lds_a, const char* lds_b) {
  s16x4 lo = __builtin_amdgcn_ds_read_tr16_b64_v4i16((lds_s16x4_ptr)lds_a);
  s16x4 hi = __builtin_amdgcn_ds_read_tr16_b64_v4i16((lds_s16x4_ptr)lds_b);
  s16x8 v = __builtin_shufflevector(lo, hi, 0, 1, 2, 3, 4, 5, 6, 7);
  return __builtin_bit_cast(bf16x8, v);
}
__device__ __forceinline__ f32x4 mfma16(bf16x8 a, bf16x8 b, f32x4 c) {
  return __builtin_amdgcn_mfma_f32_16x16x32_bf16(a, b, c, 0, 0, 0);
}
__device__ __forceinline__ float rcp_(float v) { return __builtin_amdgcn_rcpf(v); }
__device__ __forceinline__ float exp_(float v) { return __builtin_amdgcn_exp2f(v * 1.44269504088896f); }
__device__ __forceinline__ float silu_(float v) { return v * rcp_(1.0f + exp_(-v)); }
__device__ __forceinline__ float phi_(float t) { return (t > 0.0f ? t + 1.0f : exp_(t)) + 1e-6f; }

constexpr int K1_SLOT = 24576;
constexpr int K1_XN_BYTES = 114688;
__host__ __device__ constexpr int k1_slot_off(int s) { return s < 2 ? K1_XN_BYTES + K1_SLOT * s : K1_SLOT * (s - 2); }
__host__ __device__ constexpr int k1_seg_kind(int type, int seg) {
  return type == 0 ? (seg == 0 ? 0 : 1) : (seg < 2 ? 1 : 2);
}
__host__ __device__ constexpr int k1_nstage(int type) { return type == 0 ? 14 : 21; }
__host__ __device__ constexpr int k1_cnt(int type, int t) {
  return t >= k1_nstage(type) ? 0 : (k1_seg_kind(type, t / 7) == 0 ? 3 : (k1_seg_kind(type, t / 7) == 1 ? 2 : 1));
}

template <int TYPE>
__device__ __forceinline__ void k1_body(char* smem, const __hip_bfloat16* __restrict__ x,
                                        const __hip_bfloat16* __restrict__ wqkv,
                                        const __hip_bfloat16* __restrict__ w1,
                                        const __hip_bfloat16* __restrict__ w3, char* ws, int mtile,
                                        int role, const __hip_bfloat16* __restrict__ wo,
                                        const __hip_bfloat16* __restrict__ w2) {
  const int tid = threadIdx.x, w = __builtin_amdgcn_readfirstlane(tid >> 6), l = tid & 63;
  const int rg = w & 3, cg = w >> 2;
  const int g = l >> 4, q = (l & 15) >> 2, p = l & 3;
  constexpr int NSEG = TYPE == 0 ? 2 : 3;
  constexpr int NST = k1_nstage(TYPE);
  constexpr int STAMP_K = 0; STAMP(0);

  const char* src[NSEG][3];
  const int unit0 = TYPE == 0 ? role : 8 + 2 * (role - 8);
  static_for<0, NSEG>([&](auto sc) {
    constexpr int seg = sc.value;
    constexpr int kind = k1_seg_kind(TYPE, seg);
    if constexpr (kind == 1) {
      int n0 = (TYPE == 0 ? unit0 : unit0 + seg) * 64;
      int f = (l >> 4) | ((w & 1) << 2);
      int ch = (l & 15) ^ (f << 1);
      const __hip_bfloat16* base = ch < 8 ? w1 : w3;
      for (int b = 0; b < 2; ++b) {
        int row = 8 * w + 4 * b + (l >> 4);
        src[seg][b] = (const char*)(base + (size_t)row * FF + n0 + 8 * (ch & 7));
      }
    } else if constexpr (kind == 2) {
      int n0 = 1536 + 32 * (role - 8);
      int rr = l >> 3;
      int key = ((rr >> 1) & 1) | ((w & 1) << 1);
      int ch = (l & 7) ^ (key << 1);
      const __hip_bfloat16* base = ch < 4 ? w1 : w3;
      int row = 8 * w + rr;
      src[seg][0] = (const char*)(base + (size_t)row * FF + n0 + 8 * (ch & 3));
    } else {
      for (int b = 0; b < 3; ++b) {
        int cl = 64 * (3 * w + b) + l;
        int row = cl / 24, chp = cl % 24;
        int blk = chp >> 3;
        int key = ((row >> 1) & 1) | (((row >> 3) & 1) << 1);
        int cc = (chp & 7) ^ (key << 1);
        int head = 2 * role + (cc >> 2);
        int col = blk * DM + DH * head + 8 * (cc & 3);
        if (blk == 2 && head == 15) col -= 4;
        src[seg][b] = (const char*)(wqkv + (size_t)row * (3 * DM) + col);
      }
    }
  });

  auto issue = [&](auto tc) {
    constexpr int t = tc.value;
    if constexpr (t < NST) {
      constexpr int seg = t / 7, s = t % 7, kind = k1_seg_kind(TYPE, seg);
      char* slot = smem + k1_slot_off(t % 6);
      if constexpr (kind == 1) {
        glds16(src[seg][0] + (size_t)s * 64 * FF * 2, slot + (8 * w) * 256);
        glds16(src[seg][1] + (size_t)s * 64 * FF * 2, slot + (8 * w + 4) * 256);
      } else if constexpr (kind == 2) {
        glds16(src[seg][0] + (size_t)s * 64 * FF * 2, slot + w * 1024);
      } else {
        glds16(src[seg][0] + (size_t)s * 64 * (3 * DM) * 2, slot + (3 * w) * 1024);
        glds16(src[seg][1] + (size_t)s * 64 * (3 * DM) * 2, slot + (3 * w + 1) * 1024);
        glds16(src[seg][2] + (size_t)s * 64 * (3 * DM) * 2, slot + (3 * w + 2) * 1024);
      }
    }
  };

  auto issue_piece = [&](auto tc, auto ic_) {
    constexpr int t = tc.value, i = ic_.value;
    if constexpr (t < NST && i < k1_cnt(TYPE, t)) {
      constexpr int seg = t / 7, s = t % 7, kind = k1_seg_kind(TYPE, seg);
      char* slot = smem + k1_slot_off(t % 6);
      if constexpr (kind == 1) glds16(src[seg][i] + (size_t)s * 64 * FF * 2, slot + (8 * w + 4 * i) * 256);
      else if constexpr (kind == 2) glds16(src[seg][0] + (size_t)s * 64 * FF * 2, slot + w * 1024);
      else glds16(src[seg][i] + (size_t)s * 64 * (3 * DM) * 2, slot + (3 * w + i) * 1024);
    }
  };

  bf16x8 af[2][14];
  {
    const int tile = 2 * rg + cg;
    char* reg = smem + tile * 14336;
    const char* xb = (const char*)(x + (size_t)(mtile * 128 + 16 * tile) * DM);
#pragma unroll
    for (int i = 0; i < 14; ++i) {
      const int cl = 64 * i + l, row = cl / 56, chp = cl % 56;
      glds16(xb + row * 896 + ((chp ^ (row & 7)) << 4), reg + i * 1024);
    }
    wait_vm<0>();
    issue(ic<0>{});
    issue(ic<1>{});
    const int r = l & 15;
    const unsigned ra = (unsigned)(uintptr_t)(lds_void_ptr)reg + r * 896;
    const unsigned a0 = ra + ((g ^ (r & 7)) << 4), a1 = ra + (((4 + g) ^ (r & 7)) << 4);
    u32x4 raw[14];
    asm volatile(
        "ds_read_b128 %0, %14\n\tds_read_b128 %1, %15\n\t"
        "ds_read_b128 %2, %14 offset:128\n\tds_read_b128 %3, %15 offset:128\n\t"
        "ds_read_b128 %4, %14 offset:256\n\tds_read_b128 %5, %15 offset:256\n\t"
        "ds_read_b128 %6, %14 offset:384\n\tds_read_b128 %7, %15 offset:384\n\t"
        "ds_read_b128 %8, %14 offset:512\n\tds_read_b128 %9, %15 offset:512\n\t"
        "ds_read_b128 %10, %14 offset:640\n\tds_read_b128 %11, %15 offset:640\n\t"
        "ds_read_b128 %12, %14 offset:768\n\tds_read_b128 %13, %15 offset:768\n\t"
        "s_waitcnt lgkmcnt(0)"
        : "=&v"(raw[0]), "=&v"(raw[1]), "=&v"(raw[2]), "=&v"(raw[3]), "=&v"(raw[4]), "=&v"(raw[5]), "=&v"(raw[6]),
          "=&v"(raw[7]), "=&v"(raw[8]), "=&v"(raw[9]), "=&v"(raw[10]), "=&v"(raw[11]), "=&v"(raw[12]), "=&v"(raw[13])
        : "v"(a0), "v"(a1) : "memory");
    STAMP(25);
    f32x2 d[56];
    f32x2 sv[4] = {f32x2{0.f, 0.f}, f32x2{0.f, 0.f}, f32x2{0.f, 0.f}, f32x2{0.f, 0.f}};
#pragma unroll
    for (int i = 0; i < 14; ++i)
#pragma unroll
      for (int j = 0; j < 4; ++j) {
        d[i * 4 + j] = f32x2{bf_lo(raw[i][j]), bf_hi(raw[i][j])};
        sv[j] += d[i * 4 + j];
      }
    f32x2 svs = (sv[0] + sv[1]) + (sv[2] + sv[3]);
    float sum = svs[0] + svs[1];
    sum += __shfl_xor(sum, 16); sum += __shfl_xor(sum, 32);
    const float mean = sum * (1.0f / DM);
    const f32x2 mean2 = f32x2{mean, mean};
    f32x2 vq[4] = {f32x2{0.f, 0.f}, f32x2{0.f, 0.f}, f32x2{0.f, 0.f}, f32x2{0.f, 0.f}};
#pragma unroll
    for (int i = 0; i < 56; ++i) { d[i] -= mean2; vq[i & 3] += d[i] * d[i]; }
    f32x2 vv2 = (vq[0] + vq[1]) + (vq[2] + vq[3]);
    float var = vv2[0] + vv2[1];
    var += __shfl_xor(var, 16); var += __shfl_xor(var, 32);
    var = bfr(var * (1.0f / DM));
    const float den = bfr(sqrtf(bfr(var + 1e-5f)));
    const float inv = 1.0f / den;
    const float c0 = (mean - bfr(mean)) * inv;
    const f32x2 inv2 = f32x2{inv, inv}, c02 = f32x2{c0, c0};
    bf16x8 own[14];
#pragma unroll
    for (int i = 0; i < 14; ++i) {
      u32x4 o;
#pragma unroll
      for (int j = 0; j < 4; ++j) {
        f32x2 rr = d[i * 4 + j] * inv2 + c02;
        o[j] = pack2(rr[0], rr[1]);
      }
      own[i] = __builtin_bit_cast(bf16x8, o);
      *(u32x4*)(reg + i * 1024 + l * 16) = o;
    }
    STAMP(1);
    bar();
    const char* oth = smem + (2 * rg + (1 - cg)) * 14336 + l * 16;
#pragma unroll
    for (int i = 0; i < 14; ++i) {
      af[0][i] = own[i];
      af[1][i] = *(const bf16x8*)(oth + i * 1024);
    }
  }
  wait_vm<k1_cnt(TYPE, 1)>();
  bar();
  issue(ic<2>{});
  issue(ic<3>{});
  issue(ic<4>{});
  STAMP(2);

  const int tl[2] = {mtile * 8 + rg * 2 + cg, mtile * 8 + rg * 2 + 1 - cg};
  const int f_ffn = q | ((g & 1) << 2);
  const int key2 = ((q >> 1) & 1) | ((g & 1) << 1);
  const int head = 2 * role + cg;
  int off1[4], off2[4], off0[6];
#pragma unroll
  for (int c = 0; c < 4; ++c) {
    int tile = (c >> 1) * 4 + 2 * cg + (c & 1);
    off1[c] = (8 * g + q) * 256 + ((tile ^ f_ffn) << 5) + 8 * p;
    off2[c] = (8 * g + q) * 128 + ((c ^ key2) << 5) + 8 * p;
  }
#pragma unroll
  for (int c = 0; c < 6; ++c) {
    int blk = c >> 1, ct = c & 1;
    int pi = 4 * ct + p + ((blk == 2 && head == 15) ? 1 : 0);
    pi = pi > 7 ? 7 : pi;
    int cc = 4 * cg + (pi >> 1);
    off0[c] = (8 * g + q) * 384 + blk * 128 + ((cc ^ (key2 << 1)) << 4) + 8 * (pi & 1);
  }

  bf16x8 bb[2][4];
  f32x4 acc[2][6];
  auto ldB = [&](auto tc, auto pc, bf16x8* dst) {
    constexpr int t = tc.value, ph = pc.value;
    constexpr int kind = k1_seg_kind(TYPE, t / 7);
    const char* slot = smem + k1_slot_off(t % 6);
    if constexpr (kind == 1) {
#pragma unroll
      for (int c = 0; c < 4; ++c) dst[c] = tr_frag(slot + ph * 8192 + off1[c], slot + ph * 8192 + 1024 + off1[c]);
    } else if constexpr (kind == 2) {
      if (cg == 0) {
#pragma unroll
        for (int c = 0; c < 4; ++c) dst[c] = tr_frag(slot + ph * 4096 + off2[c], slot + ph * 4096 + 512 + off2[c]);
      }
    } else {
      constexpr int ks = ph >> 1, c0 = 3 * (ph & 1);
#pragma unroll
      for (int c = 0; c < 3; ++c)
        dst[c] = tr_frag(slot + ks * 12288 + off0[c0 + c], slot + ks * 12288 + 1536 + off0[c0 + c]);
    }
  };
  auto mma = [&](auto tc, auto pc, const bf16x8* b) {
    constexpr int t = tc.value, ph = pc.value, s = t % 7;
    constexpr int kind = k1_seg_kind(TYPE, t / 7);
    const f32x4 z = f32x4{0.f, 0.f, 0.f, 0.f};
    if constexpr (kind == 1) {
      constexpr bool first = (s == 0 && ph == 0);
#pragma unroll
      for (int c = 0; c < 4; ++c) {
        acc[0][c] = mfma16(b[c], af[0][2 * s + ph], first ? z : acc[0][c]);
        acc[1][c] = mfma16(b[c], af[1][2 * s + ph], first ? z : acc[1][c]);
      }
    } else if constexpr (kind == 2) {
      constexpr bool first = (s == 0 && ph == 0);
      if (cg == 0) {
#pragma unroll
        for (int c = 0; c < 4; ++c) {
          acc[0][c] = mfma16(b[c], af[0][2 * s + ph], first ? z : acc[0][c]);
          acc[1][c] = mfma16(b[c], af[1][2 * s + ph], first ? z : acc[1][c]);
        }
      }
    } else {
      constexpr int ks = ph >> 1, c0 = 3 * (ph & 1);
      constexpr bool first = (s == 0 && ks == 0);
#pragma unroll
      for (int c = 0; c < 3; ++c) {
        if (c0 + c < 2) {
          acc[0][c0 + c] = mfma16(b[c], af[0][2 * s + ks], first ? z : acc[0][c0 + c]);
          acc[1][c0 + c] = mfma16(b[c], af[1][2 * s + ks], first ? z : acc[1][c0 + c]);
        } else {
          acc[0][c0 + c] = mfma16(af[0][2 * s + ks], b[c], first ? z : acc[0][c0 + c]);
          acc[1][c0 + c] = mfma16(af[1][2 * s + ks], b[c], first ? z : acc[1][c0 + c]);
        }
      }
    }
  };
  auto ffn_epi = [&](int kstep) {
#pragma unroll
    for (int rt = 0; rt < 2; ++rt) {
      float h[8];
#pragma unroll
      for (int c = 0; c < 2; ++c)
#pragma unroll
        for (int j = 0; j < 4; ++j) h[c * 4 + j] = silu_(acc[rt][c][j]) * acc[rt][2 + c][j];
      u32x4 o = {pack2(h[0], h[1]), pack2(h[2], h[3]), pack2(h[4], h[5]), pack2(h[6], h[7])};
      *(u32x4*)(ws + WS_H + ((size_t)(tl[rt] * 56 + kstep) * 64 + l) * 16) = o;
    }
  };

  ldB(ic<0>{}, ic<0>{}, bb[0]);

  static_for<0, NST>([&](auto tc) {
    constexpr int t = tc.value;
    constexpr int seg = t / 7, kind = k1_seg_kind(TYPE, seg);
    wait_vm<k1_cnt(TYPE, t + 2) + k1_cnt(TYPE, t + 3) + k1_cnt(TYPE, t + 4)>();
    bar(); STAMP(3 + t);
    if constexpr (kind == 2 && t % 7 == 0) {
      if (cg == 1) {
        const int wid = ((mtile * 8 + (role - 8)) * 4 + rg);
        const char* pw = (const char*)w2 + (size_t)wid * 3136 + l * 16;
        const char* po = (const char*)wo + (size_t)wid * 784 + l * 16;
        u32x4 t0, t1, t2, t3, t4;
        asm volatile("global_load_dwordx4 %0, %5, off\n\tglobal_load_dwordx4 %1, %5, off offset:1024\n\t"
                     "global_load_dwordx4 %2, %5, off offset:2048\n\tglobal_load_dwordx4 %3, %5, off offset:2112\n\t"
                     "global_load_dwordx4 %4, %6, off\n\ts_waitcnt vmcnt(0)"
                     : "=&v"(t0), "=&v"(t1), "=&v"(t2), "=&v"(t3), "=&v"(t4) : "v"(pw), "v"(l < 49 ? po : (const char*)wo) : "memory");
      }
    }
    constexpr int NPH = kind == 0 ? 4 : 2;
    static_for<0, NPH>([&](auto pc) {
      constexpr int ph = pc.value;
      if constexpr (ph > 0) __builtin_amdgcn_s_waitcnt(0xC07F);
      if constexpr (ph + 1 < NPH) ldB(tc, ic<ph + 1>{}, bb[(ph + 1) & 1]);
      else if constexpr (t + 1 < NST) ldB(ic<t + 1>{}, ic<0>{}, bb[0]);
      issue_piece(ic<t + 5>{}, pc);
      if constexpr (NPH == 2 && ph == 1) issue_piece(ic<t + 5>{}, ic<2>{});
      __builtin_amdgcn_sched_barrier(0);
      mma(tc, pc, bb[ph & 1]);
      __builtin_amdgcn_sched_barrier(0);
    });
    if constexpr (t % 7 == 6) {
      if constexpr (kind == 1) {
        ffn_epi((TYPE == 0 ? unit0 : unit0 + seg) * 2 + cg);
      } else if constexpr (kind == 2) {
        if (cg == 0) ffn_epi(48 + (role - 8));
      } else {
#pragma unroll
        for (int rt = 0; rt < 2; ++rt) {
          float v[8];
#pragma unroll
          for (int j = 0; j < 4; ++j) {
            v[j] = phi_(acc[rt][0][j]);
            float t1 = phi_(acc[rt][1][j]);
            v[4 + j] = g == 3 ? 0.f : t1;
          }
          u32x4 o = {pack2(v[0], v[1]), pack2(v[2], v[3]), pack2(v[4], v[5]), pack2(v[6], v[7])};
          *(u32x4*)(ws + WS_PQ + ((size_t)(tl[rt] * 16 + head) * 64 + l) * 16) = o;
        }
        bf16x8 pk[2], vv[2];
#pragma unroll
        for (int ct = 0; ct < 2; ++ct) {
          u32x4 a = {pack2(phi_(acc[0][2 + ct][0]), phi_(acc[0][2 + ct][1])),
                     pack2(phi_(acc[0][2 + ct][2]), phi_(acc[0][2 + ct][3])),
                     pack2(phi_(acc[1][2 + ct][0]), phi_(acc[1][2 + ct][1])),
                     pack2(phi_(acc[1][2 + ct][2]), phi_(acc[1][2 + ct][3]))};
          pk[ct] = __builtin_bit_cast(bf16x8, a);
          u32x4 b = {pack2(acc[0][4 + ct][0], acc[0][4 + ct][1]), pack2(acc[0][4 + ct][2], acc[0][4 + ct][3]),
                     pack2(acc[1][4 + ct][0], acc[1][4 + ct][1]), pack2(acc[1][4 + ct][2], acc[1][4 + ct][3])};
          if (ct == 1 && (l & 15) == 12) b = u32x4{0x3f803f80u, 0x3f803f80u, 0x3f803f80u, 0x3f803f80u};
          vv[ct] = __builtin_bit_cast(bf16x8, b);
        }
        f32x4 kvd[4];
#pragma unroll
        for (int dt = 0; dt < 2; ++dt)
#pragma unroll
          for (int et = 0; et < 2; ++et) kvd[dt * 2 + et] = mfma16(pk[dt], vv[et], f32x4{0.f, 0.f, 0.f, 0.f});
        bar();
        {
          char* sc = smem + k1_slot_off(w >> 1) + 16384 + (w & 1) * 4096;
#pragma unroll
          for (int i = 0; i < 4; ++i) *(f32x4*)(sc + i * 1024 + l * 16) = kvd[i];
        }
        bar();
        {
          const int hh = tid >> 8, idx = tid & 255;
          f32x4 sum = f32x4{0.f, 0.f, 0.f, 0.f};
#pragma unroll
          for (int r = 0; r < 4; ++r) {
            int ww = hh * 4 + r;
            sum += *(const f32x4*)(smem + k1_slot_off(ww >> 1) + 16384 + (ww & 1) * 4096 + idx * 16);
          }
          *(f32x4*)(ws + WS_KVP + ((size_t)(mtile * 16 + 2 * role + hh) * 256 + idx) * 16) = sum;
        }
      }
    }
  });
  STAMP(31);
}

__global__ __launch_bounds__(512, 2) void k1_kernel(const __hip_bfloat16* __restrict__ x,
                                                    const __hip_bfloat16* __restrict__ wqkv,
                                                    const __hip_bfloat16* __restrict__ w1,
                                                    const __hip_bfloat16* __restrict__ w3, char* ws,
                                                    const __hip_bfloat16* __restrict__ wo,
                                                    const __hip_bfloat16* __restrict__ w2) {
  __shared__ __attribute__((aligned(16))) char smem[163840];
  const int b = blockIdx.x;
  const int xc = b & 7, ix = b >> 3;
  const int role = (xc & 1) * 8 + (ix & 7), mtile = 4 * (xc >> 1) + (ix >> 3);
  if (b == 0 && threadIdx.x == 0) *(unsigned*)(ws + WS_FLAG) = 0u;
  if (role < 8) k1_body<0>(smem, x, wqkv, w1, w3, ws, mtile, role, wo, w2);
  else k1_body<1>(smem, x, wqkv, w1, w3, ws, mtile, role, wo, w2);
}

constexpr int K2_SLOT = 32768;
constexpr int K2_NSLOT = 4;
constexpr int K2_KV = K2_NSLOT * K2_SLOT;
constexpr int K2_NGEMM = 224;

constexpr int K2_PW = 10, K2_TF = 2;

__device__ __forceinline__ bool k2_flag_ready(unsigned lds0) {
  unsigned f;
  asm volatile("ds_read_b32 %0, %1\n\ts_waitcnt lgkmcnt(0)" : "=&v"(f) : "v"(lds0 + K2_KV + 31 * 1024) : "memory");
  return __builtin_amdgcn_readfirstlane(f) >= 16u;
}

__device__ __forceinline__ void k2_loader(char* smem, const __hip_bfloat16* __restrict__ wo,
                                          const __hip_bfloat16* __restrict__ w2, char* ws, int j, int mt, int n0) {
  const int l = threadIdx.x & 63;
  const int chs = (l & 7) ^ (((l >> 4) & 3) << 1);
  const char* src_w2 = (const char*)(w2 + (size_t)(32 * j + (l >> 3)) * DM + n0 + 8 * chs);
  const char* src_wo = (const char*)(wo + (size_t)(l >> 3) * DM + n0 + 8 * chs);
  const char* src_h = ws + WS_H + ((size_t)((mt * 4 + j) * 56) * 64 + l) * 16;
  const unsigned lds0 = (unsigned)(uintptr_t)(lds_void_ptr)smem;
  auto issue = [&](auto tc) {
    constexpr int t = tc.value;
    char* slot = smem + (t % K2_NSLOT) * K2_SLOT;
    if constexpr (t == K2_PW || t == K2_PW + 1) {
#pragma unroll
      for (int i = 0; i < 8; ++i) {
        int row0 = 224 * (t - K2_PW) + 8 * (8 * j + i);
        int row = row0 + (l >> 3);
        const char* sp = src_wo + (size_t)row0 * DM * 2;
        if (row > 447) sp -= (size_t)(row - 447) * DM * 2;
        glds16(sp, slot + (8 * j + i) * 1024);
      }
    } else if constexpr (t < 16) {
      constexpr int f = t < K2_PW ? t : t - 2;
#pragma unroll
      for (int i = 0; i < 4; ++i) glds16(src_w2 + (size_t)(f * 128 + 8 * i) * DM * 2, slot + (4 * j + i) * 1024);
#pragma unroll
      for (int i = 0; i < 4; ++i) glds16(src_h + (size_t)(f * 4 + i) * 1024, slot + 16384 + (j * 4 + i) * 1024);
    }
  };
  auto issue_kv = [&]() {
#pragma unroll
    for (int i = 0; i < 8; ++i)
      __builtin_amdgcn_global_load_lds((gbl_cvoid_ptr)(ws + WS_KVR + (size_t)((8 * j + i) * 64 + l) * 16),
                                       (lds_void_ptr)(smem + K2_KV + (8 * j + i) * 1024), 16, 0, 16);
  };
  static_for<0, K2_NSLOT - 1>([&](auto tc) { issue(tc); });
  bool kv_ready = false;
  static_for<0, 16>([&](auto tc) {
    constexpr int t = tc.value;
    wait_vm<8 * ((K2_NSLOT - 2) < (15 - t) ? (K2_NSLOT - 2) : (15 - t))>();
    bar();
    if constexpr (t == K2_PW) {
      if (!kv_ready) {
        unsigned spins = 0;
        while (__hip_atomic_load((unsigned*)(ws + WS_FLAG), __ATOMIC_RELAXED, __HIP_MEMORY_SCOPE_AGENT) < 16u) {
          __builtin_amdgcn_s_sleep(2);
          if (++spins > (1u << 24)) break;
        }
        asm volatile("" ::: "memory");
        issue_kv();
        wait_vm<0>();
        bar();
      }
    }
    issue(ic<t + K2_NSLOT - 1>{});
    if constexpr (t == K2_TF) {
      if (j == 0)
        __builtin_amdgcn_global_load_lds((gbl_cvoid_ptr)(ws + WS_FLAG), (lds_void_ptr)(smem + K2_KV + 31 * 1024), 4, 0, 16);
    }
    if constexpr (t == K2_TF + 3) kv_ready = k2_flag_ready(lds0);
    if constexpr (t == K2_TF + 4) {
      if (kv_ready) issue_kv();
    }
  });
  bar();
}

__device__ __forceinline__ void k2_gemm(char* smem, const __hip_bfloat16* __restrict__ x, char* ws,
                                        __hip_bfloat16* __restrict__ out, int w, int mt, int n0) {
  const int tid = threadIdx.x, l = tid & 63;
  const int rt = w & 3, kg = w >> 2;
  const int g = l >> 4, q = (l & 15) >> 2, p = l & 3;
  constexpr int STAMP_K = 1; STAMP(0);

  u32x4 pqf[8];
#pragma unroll
  for (int hh = 0; hh < 8; ++hh)
    pqf[hh] = *(const u32x4*)(ws + WS_PQ + ((size_t)((mt * 4 + rt) * 16 + 2 * hh + kg) * 64 + l) * 16);
  const int orow = mt * 64 + rt * 16 + (l & 15);
  u32x2 xr[2];
#pragma unroll
  for (int i = 0; i < 2; ++i) xr[i] = *(const u32x2*)(x + (size_t)orow * DM + n0 + (2 * kg + i) * 16 + 4 * g);
  asm volatile("" ::: "memory");

  f32x4 acc1[4], acc2[4];
#pragma unroll
  for (int c = 0; c < 4; ++c) { acc1[c] = f32x4{0.f, 0.f, 0.f, 0.f}; acc2[c] = acc1[c]; }

  int offa[4], offwa[4], offwb[4];
#pragma unroll
  for (int c = 0; c < 4; ++c) {
    int r1 = 4 * g + q;
    offa[c] = r1 * 128 + ((c ^ ((r1 >> 1) & 3)) << 5) + 8 * p;
    int r2 = 16 + 4 * (g > 2 ? 2 : g) + q;
    offwa[c] = (28 * kg + r1) * 128 + ((c ^ ((2 * kg + (r1 >> 1)) & 3)) << 5) + 8 * p;
    offwb[c] = (28 * kg + r2) * 128 + ((c ^ ((2 * kg + (r2 >> 1)) & 3)) << 5) + 8 * p;
  }
  bool kv_ready = false;
  const unsigned lds0 = (unsigned)(uintptr_t)(lds_void_ptr)smem;
  bf16x8 of[8];

  static_for<0, 16>([&](auto tc) {
    constexpr int t = tc.value;
    bar(); STAMP(1 + t);
    const char* slot = smem + (t % K2_NSLOT) * K2_SLOT;
    if constexpr (t == K2_PW || t == K2_PW + 1) {
      if constexpr (t == K2_PW) {
        if (!kv_ready) bar();
        f32x4 n0v[8], n1v[8];
#pragma unroll
        for (int hh = 0; hh < 8; ++hh) {
          bf16x8 pq = __builtin_bit_cast(bf16x8, pqf[hh]);
          const char* kp = smem + K2_KV + (2 * hh + kg) * 2048 + l * 16;
          n0v[hh] = mfma16(*(const bf16x8*)kp, pq, f32x4{0.f, 0.f, 0.f, 0.f});
          n1v[hh] = mfma16(*(const bf16x8*)(kp + 1024), pq, f32x4{0.f, 0.f, 0.f, 0.f});
        }
        float den[8];
#pragma unroll
        for (int hh = 0; hh < 8; ++hh) den[hh] = __shfl(n1v[hh][0], (l & 15) + 48);
#pragma unroll
        for (int hh = 0; hh < 8; ++hh) {
          const float inv = rcp_(den[hh] + 1e-6f);
          const float invm = g == 3 ? 0.f : inv;
          u32x4 ov = {pack2(n0v[hh][0] * inv, n0v[hh][1] * inv), pack2(n0v[hh][2] * inv, n0v[hh][3] * inv),
                      pack2(n1v[hh][0] * invm, n1v[hh][1] * invm), pack2(n1v[hh][2] * invm, n1v[hh][3] * invm)};
          of[hh] = __builtin_bit_cast(bf16x8, ov);
        }
      }
#pragma unroll
      for (int i = 0; i < 4; ++i) {
        const int hh = (t - K2_PW) * 4 + i;
#pragma unroll
        for (int c = 0; c < 4; ++c) {
          bf16x8 bfg = tr_frag(slot + i * 7168 + offwa[c], slot + i * 7168 + offwb[c]);
          acc2[c] = mfma16(bfg, of[hh], acc2[c]);
        }
      }
    } else {
      const bf16x8 hf0 = *(const bf16x8*)(slot + 16384 + (rt * 4 + kg) * 1024 + l * 16);
      const bf16x8 hf1 = *(const bf16x8*)(slot + 16384 + (rt * 4 + kg + 2) * 1024 + l * 16);
      bf16x8 bA[4], bB[4];
#pragma unroll
      for (int c = 0; c < 4; ++c) bA[c] = tr_frag(slot + kg * 4096 + offa[c], slot + kg * 4096 + 2048 + offa[c]);
#pragma unroll
      for (int c = 0; c < 4; ++c) bB[c] = tr_frag(slot + (kg + 2) * 4096 + offa[c], slot + (kg + 2) * 4096 + 2048 + offa[c]);
#pragma unroll
      for (int c = 0; c < 4; ++c) acc1[c] = mfma16(bA[c], hf0, acc1[c]);
#pragma unroll
      for (int c = 0; c < 4; ++c) acc1[c] = mfma16(bB[c], hf1, acc1[c]);
    }
    if constexpr (t == K2_TF + 3) kv_ready = k2_flag_ready(lds0);
  });

  STAMP(17);
  {
    char* ex = smem + K2_SLOT + w * 4096;
    *(f32x4*)(ex + 0 * 1024 + l * 16) = kg ? acc1[0] : acc1[2];
    *(f32x4*)(ex + 1 * 1024 + l * 16) = kg ? acc1[1] : acc1[3];
    *(f32x4*)(ex + 2 * 1024 + l * 16) = kg ? acc2[0] : acc2[2];
    *(f32x4*)(ex + 3 * 1024 + l * 16) = kg ? acc2[1] : acc2[3];
  }
  bar();
  {
    const char* ex = smem + K2_SLOT + (w ^ 4) * 4096;
#pragma unroll
    for (int i = 0; i < 2; ++i) {
      f32x4 a1 = (kg ? acc1[2 + i] : acc1[i]) + *(const f32x4*)(ex + i * 1024 + l * 16);
      f32x4 a2 = (kg ? acc2[2 + i] : acc2[i]) + *(const f32x4*)(ex + (2 + i) * 1024 + l * 16);
      const int col = n0 + (2 * kg + i) * 16 + 4 * g;
      float xv[4] = {bf_lo(xr[i][0]), bf_hi(xr[i][0]), bf_lo(xr[i][1]), bf_hi(xr[i][1])};
      float r[4];
#pragma unroll
      for (int jj = 0; jj < 4; ++jj) r[jj] = xv[jj] + bfr(a2[jj]) * bfr(silu_(bfr(a1[jj])));
      u32x2 ov = {pack2(r[0], r[1]), pack2(r[2], r[3])};
      *(u32x2*)(out + (size_t)orow * DM + col) = ov;
    }
  }
  STAMP(18);
}

__global__ __launch_bounds__(768, 3) void k2_kernel(const __hip_bfloat16* __restrict__ x,
                                                    const __hip_bfloat16* __restrict__ wo,
                                                    const __hip_bfloat16* __restrict__ w2,
                                                    char* ws, __hip_bfloat16* __restrict__ out) {
  __shared__ __attribute__((aligned(16))) char smem[K2_NSLOT * K2_SLOT + 32768];
  const int tid = threadIdx.x, l = tid & 63;
  const int b = blockIdx.x;
  if (b >= K2_NGEMM) {
    const int head = b - K2_NGEMM;
    if (tid < 128) {
      const int et = tid >> 6;
      f32x4 s0 = f32x4{0.f, 0.f, 0.f, 0.f}, s1 = s0;
#pragma unroll
      for (int m = 0; m < 16; ++m) {
        const char* pb = ws + WS_KVP + ((size_t)(m * 16 + head) * 256) * 16;
        s0 += *(const f32x4*)(pb + ((0 * 2 + et) * 64 + l) * 16);
        s1 += *(const f32x4*)(pb + ((1 * 2 + et) * 64 + l) * 16);
      }
      if ((l >> 4) == 3) s1 = f32x4{0.f, 0.f, 0.f, 0.f};
      if (et == 1 && (l & 15) > 12) { s0 = f32x4{0.f, 0.f, 0.f, 0.f}; s1 = s0; }
      u32x4 o = {pack2(s0[0], s0[1]), pack2(s0[2], s0[3]), pack2(s1[0], s1[1]), pack2(s1[2], s1[3])};
      const __amdgpu_buffer_rsrc_t rs = __builtin_amdgcn_make_buffer_rsrc(ws + WS_KVR, 0, 32768, 0x00020000);
      __builtin_amdgcn_raw_buffer_store_b128(o, rs, (unsigned)(((head * 2 + et) * 64 + l) * 16), 0, 16);
      asm volatile("s_waitcnt vmcnt(0)" ::: "memory");
    }
    __syncthreads();
    if (tid == 0)
      __hip_atomic_fetch_add((unsigned*)(ws + WS_FLAG), 1u, __ATOMIC_RELAXED, __HIP_MEMORY_SCOPE_AGENT);
    return;
  }
  const int xcd = b & 7, idx = b >> 3;
  const int mt = 4 * xcd + idx / 7, n0 = (idx % 7) * 64;
  const int w = __builtin_amdgcn_readfirstlane(tid >> 6);
  if (w >= 8) k2_loader(smem, wo, w2, ws, w - 8, mt, n0);
  else k2_gemm(smem, x, ws, out, w, mt, n0);
}

extern "C" void kernel_launch(void* const* d_in, const int* in_sizes, int n_in, void* d_out, int out_size,
                              void* d_ws, size_t ws_size, hipStream_t stream) {
  const __hip_bfloat16* x = (const __hip_bfloat16*)d_in[0];
  const __hip_bfloat16* wqkv = (const __hip_bfloat16*)d_in[1];
  const __hip_bfloat16* wo = (const __hip_bfloat16*)d_in[2];
  const __hip_bfloat16* w1 = (const __hip_bfloat16*)d_in[4];
  const __hip_bfloat16* w3 = (const __hip_bfloat16*)d_in[5];
  const __hip_bfloat16* w2 = (const __hip_bfloat16*)d_in[6];
  char* ws = (char*)d_ws;
  k1_kernel<<<256, 512, 0, stream>>>(x, wqkv, w1, w3, ws, wo, w2);
  k2_kernel<<<K2_NGEMM + 16, 768, 0, stream>>>(x, wo, w2, ws, (__hip_bfloat16*)d_out);
}
```

```cpp
#include <hip/hip_runtime.h>
#include <hip/hip_bf16.h>
#include <stdint.h>
#include <type_traits>

typedef __attribute__((ext_vector_type(8))) __bf16 bf16x8;
typedef __attribute__((ext_vector_type(4))) short s16x4;
typedef __attribute__((ext_vector_type(8))) short s16x8;
typedef __attribute__((ext_vector_type(4))) float f32x4;
typedef __attribute__((ext_vector_type(2))) float f32x2;
typedef __attribute__((ext_vector_type(4))) unsigned u32x4;
typedef __attribute__((ext_vector_type(2))) unsigned u32x2;
typedef __attribute__((address_space(3))) s16x4* lds_s16x4_ptr;
typedef __attribute__((address_space(3))) void* lds_void_ptr;
typedef const __attribute__((address_space(1))) void* gbl_cvoid_ptr;

constexpr int SEQ = 2048, DM = 448, FF = 1792, NH = 16, DH = 28;

constexpr size_t WS_H    = 0;
constexpr size_t WS_PQ   = 7340032;
constexpr size_t WS_KVP  = WS_PQ + 2097152;
constexpr size_t WS_KVR  = WS_KVP + 1048576;
constexpr size_t WS_FLAG = WS_KVR + 32768;

#define STAMP(slot) do{}while(0)

template <int I, int N, class Fn>
__device__ __forceinline__ void static_for(Fn&& f) {
  if constexpr (I < N) { f(std::integral_constant<int, I>{}); static_for<I + 1, N>(f); }
}
template <int N> using ic = std::integral_constant<int, N>;
template <int N> __device__ __forceinline__ void wait_vm() {
  asm volatile("" ::: "memory");
  __builtin_amdgcn_s_waitcnt((N & 0xF) | (7 << 4) | (0xF << 8) | ((N >> 4) << 14));
  asm volatile("" ::: "memory");
}
__device__ __forceinline__ void bar() {
  asm volatile("" ::: "memory");
  __builtin_amdgcn_s_waitcnt(0xC07F);
  __builtin_amdgcn_s_barrier();
  asm volatile("" ::: "memory");
}

__device__ __forceinline__ float bfr(float f) { return (float)(__bf16)f; }
__device__ __forceinline__ float bf_lo(unsigned u) { return __uint_as_float(u << 16); }
__device__ __forceinline__ float bf_hi(unsigned u) { return __uint_as_float(u & 0xffff0000u); }
__device__ __forceinline__ unsigned pack2(float a, float b) {
  typedef __attribute__((ext_vector_type(2))) __bf16 bf16x2;
  bf16x2 v; v[0] = (__bf16)a; v[1] = (__bf16)b;
  return __builtin_bit_cast(unsigned, v);
}
__device__ __forceinline__ void glds16(const void* g, void* l) {
  __builtin_amdgcn_global_load_lds((gbl_cvoid_ptr)g, (lds_void_ptr)l, 16, 0, 0);
}
__device__ __forceinline__ bf16x8 tr_frag(const char* lds_a, const char* lds_b) {
  s16x4 lo = __builtin_amdgcn_ds_read_tr16_b64_v4i16((lds_s16x4_ptr)lds_a);
  s16x4 hi = __builtin_amdgcn_ds_read_tr16_b64_v4i16((lds_s16x4_ptr)lds_b);
  s16x8 v = __builtin_shufflevector(lo, hi, 0, 1, 2, 3, 4, 5, 6, 7);
  return __builtin_bit_cast(bf16x8, v);
}
__device__ __forceinline__ f32x4 mfma16(bf16x8 a, bf16x8 b, f32x4 c) {
  return __builtin_amdgcn_mfma_f32_16x16x32_bf16(a, b, c, 0, 0, 0);
}
__device__ __forceinline__ float rcp_(float v) { return __builtin_amdgcn_rcpf(v); }
__device__ __forceinline__ float exp_(float v) { return __builtin_amdgcn_exp2f(v * 1.44269504088896f); }
__device__ __forceinline__ float silu_(float v) { return v * rcp_(1.0f + exp_(-v)); }
__device__ __forceinline__ float phi_(float t) { return (t > 0.0f ? t + 1.0f : exp_(t)) + 1e-6f; }

constexpr int K1_SLOT = 24576;
constexpr int K1_XN_BYTES = 114688;
__host__ __device__ constexpr int k1_slot_off(int s) { return s < 2 ? K1_XN_BYTES + K1_SLOT * s : K1_SLOT * (s - 2); }
__host__ __device__ constexpr int k1_seg_kind(int type, int seg) {
  return type == 0 ? (seg == 0 ? 0 : 1) : (seg < 2 ? 1 : 2);
}
__host__ __device__ constexpr int k1_nstage(int type) { return type == 0 ? 14 : 21; }
__host__ __device__ constexpr int k1_cnt(int type, int t) {
  return t >= k1_nstage(type) ? 0 : (k1_seg_kind(type, t / 7) == 0 ? 3 : (k1_seg_kind(type, t / 7) == 1 ? 2 : 1));
}

template <int TYPE>
__device__ __forceinline__ void k1_body(char* smem, const __hip_bfloat16* __restrict__ x,
                                        const __hip_bfloat16* __restrict__ wqkv,
                                        const __hip_bfloat16* __restrict__ w1,
                                        const __hip_bfloat16* __restrict__ w3, char* ws, int mtile,
                                        int role, const __hip_bfloat16* __restrict__ wo,
                                        const __hip_bfloat16* __restrict__ w2) {
  const int tid = threadIdx.x, w = __builtin_amdgcn_readfirstlane(tid >> 6), l = tid & 63;
  const int rg = w & 3, cg = w >> 2;
  const int g = l >> 4, q = (l & 15) >> 2, p = l & 3;
  constexpr int NSEG = TYPE == 0 ? 2 : 3;
  constexpr int NST = k1_nstage(TYPE);
  constexpr int STAMP_K = 0; STAMP(0);

  const char* src[NSEG][3];
  const int unit0 = TYPE == 0 ? role : 8 + 2 * (role - 8);
  static_for<0, NSEG>([&](auto sc) {
    constexpr int seg = sc.value;
    constexpr int kind = k1_seg_kind(TYPE, seg);
    if constexpr (kind == 1) {
      int n0 = (TYPE == 0 ? unit0 : unit0 + seg) * 64;
      int f = (l >> 4) | ((w & 1) << 2);
      int ch = (l & 15) ^ (f << 1);
      const __hip_bfloat16* base = ch < 8 ? w1 : w3;
      for (int b = 0; b < 2; ++b) {
        int row = 8 * w + 4 * b + (l >> 4);
        src[seg][b] = (const char*)(base + (size_t)row * FF + n0 + 8 * (ch & 7));
      }
    } else if constexpr (kind == 2) {
      int n0 = 1536 + 32 * (role - 8);
      int rr = l >> 3;
      int key = ((rr >> 1) & 1) | ((w & 1) << 1);
      int ch = (l & 7) ^ (key << 1);
      const __hip_bfloat16* base = ch < 4 ? w1 : w3;
      int row = 8 * w + rr;
      src[seg][0] = (const char*)(base + (size_t)row * FF + n0 + 8 * (ch & 3));
    } else {
      for (int b = 0; b < 3; ++b) {
        int cl = 64 * (3 * w + b) + l;
        int row = cl / 24, chp = cl % 24;
        int blk = chp >> 3;
        int key = ((row >> 1) & 1) | (((row >> 3) & 1) << 1);
        int cc = (chp & 7) ^ (key << 1);
        int head = 2 * role + (cc >> 2);
        int col = blk * DM + DH * head + 8 * (cc & 3);
        if (blk == 2 && head == 15) col -= 4;
        src[seg][b] = (const char*)(wqkv + (size_t)row * (3 * DM) + col);
      }
    }
  });

  auto issue = [&](auto tc) {
    constexpr int t = tc.value;
    if constexpr (t < NST) {
      constexpr int seg = t / 7, s = t % 7, kind = k1_seg_kind(TYPE, seg);
      char* slot = smem + k1_slot_off(t % 6);
      if constexpr (kind == 1) {
        glds16(src[seg][0] + (size_t)s * 64 * FF * 2, slot + (8 * w) * 256);
        glds16(src[seg][1] + (size_t)s * 64 * FF * 2, slot + (8 * w + 4) * 256);
      } else if constexpr (kind == 2) {
        glds16(src[seg][0] + (size_t)s * 64 * FF * 2, slot + w * 1024);
      } else {
        glds16(src[seg][0] + (size_t)s * 64 * (3 * DM) * 2, slot + (3 * w) * 1024);
        glds16(src[seg][1] + (size_t)s * 64 * (3 * DM) * 2, slot + (3 * w + 1) * 1024);
        glds16(src[seg][2] + (size_t)s * 64 * (3 * DM) * 2, slot + (3 * w + 2) * 1024);
      }
    }
  };

  auto issue_piece = [&](auto tc, auto ic_) {
    constexpr int t = tc.value, i = ic_.value;
    if constexpr (t < NST && i < k1_cnt(TYPE, t)) {
      constexpr int seg = t / 7, s = t % 7, kind = k1_seg_kind(TYPE, seg);
      char* slot = smem + k1_slot_off(t % 6);
      if constexpr (kind == 1) glds16(src[seg][i] + (size_t)s * 64 * FF * 2, slot + (8 * w + 4 * i) * 256);
      else if constexpr (kind == 2) glds16(src[seg][0] + (size_t)s * 64 * FF * 2, slot + w * 1024);
      else glds16(src[seg][i] + (size_t)s * 64 * (3 * DM) * 2, slot + (3 * w + i) * 1024);
    }
  };

  bf16x8 af[2][14];
  unsigned pf[4];
  {
    const int tile = 2 * rg + cg;
    char* reg = smem + tile * 14336;
    const char* xb = (const char*)(x + (size_t)(mtile * 128 + 16 * tile) * DM);
#pragma unroll
    for (int i = 0; i < 14; ++i) {
      const int cl = 64 * i + l, row = cl / 56, chp = cl % 56;
      glds16(xb + row * 896 + ((chp ^ (row & 7)) << 4), reg + i * 1024);
    }
    {
      const int wid = blockIdx.x * 8 + w;
#pragma unroll
      for (int k = 0; k < 4; ++k) {
        int pc = wid + 2048 * k;
        pc = pc < 6272 ? pc : 6271;
        const char* bp = pc < 1568 ? (const char*)w1 + (size_t)pc * 1024
                       : pc < 3136 ? (const char*)w3 + (size_t)(pc - 1568) * 1024
                       : pc < 4312 ? (const char*)wqkv + (size_t)(pc - 3136) * 1024
                       : pc < 5880 ? (const char*)w2 + (size_t)(pc - 4312) * 1024
                                   : (const char*)wo + (size_t)(pc - 5880) * 1024;
        asm volatile("global_load_dword %0, %1, off" : "=v"(pf[k]) : "v"(bp + l * 16) : "memory");
      }
    }
    wait_vm<4>();
    issue(ic<0>{});
    issue(ic<1>{});
    const int r = l & 15;
    const unsigned ra = (unsigned)(uintptr_t)(lds_void_ptr)reg + r * 896;
    const unsigned a0 = ra + ((g ^ (r & 7)) << 4), a1 = ra + (((4 + g) ^ (r & 7)) << 4);
    u32x4 raw[14];
    asm volatile(
        "ds_read_b128 %0, %14\n\tds_read_b128 %1, %15\n\t"
        "ds_read_b128 %2, %14 offset:128\n\tds_read_b128 %3, %15 offset:128\n\t"
        "ds_read_b128 %4, %14 offset:256\n\tds_read_b128 %5, %15 offset:256\n\t"
        "ds_read_b128 %6, %14 offset:384\n\tds_read_b128 %7, %15 offset:384\n\t"
        "ds_read_b128 %8, %14 offset:512\n\tds_read_b128 %9, %15 offset:512\n\t"
        "ds_read_b128 %10, %14 offset:640\n\tds_read_b128 %11, %15 offset:640\n\t"
        "ds_read_b128 %12, %14 offset:768\n\tds_read_b128 %13, %15 offset:768\n\t"
        "s_waitcnt lgkmcnt(0)"
        : "=&v"(raw[0]), "=&v"(raw[1]), "=&v"(raw[2]), "=&v"(raw[3]), "=&v"(raw[4]), "=&v"(raw[5]), "=&v"(raw[6]),
          "=&v"(raw[7]), "=&v"(raw[8]), "=&v"(raw[9]), "=&v"(raw[10]), "=&v"(raw[11]), "=&v"(raw[12]), "=&v"(raw[13])
        : "v"(a0), "v"(a1) : "memory");
    STAMP(25);
    f32x2 d[56];
    f32x2 sv[4] = {f32x2{0.f, 0.f}, f32x2{0.f, 0.f}, f32x2{0.f, 0.f}, f32x2{0.f, 0.f}};
#pragma unroll
    for (int i = 0; i < 14; ++i)
#pragma unroll
      for (int j = 0; j < 4; ++j) {
        d[i * 4 + j] = f32x2{bf_lo(raw[i][j]), bf_hi(raw[i][j])};
        sv[j] += d[i * 4 + j];
      }
    f32x2 svs = (sv[0] + sv[1]) + (sv[2] + sv[3]);
    float sum = svs[0] + svs[1];
    sum += __shfl_xor(sum, 16); sum += __shfl_xor(sum, 32);
    const float mean = sum * (1.0f / DM);
    const f32x2 mean2 = f32x2{mean, mean};
    f32x2 vq[4] = {f32x2{0.f, 0.f}, f32x2{0.f, 0.f}, f32x2{0.f, 0.f}, f32x2{0.f, 0.f}};
#pragma unroll
    for (int i = 0; i < 56; ++i) { d[i] -= mean2; vq[i & 3] += d[i] * d[i]; }
    f32x2 vv2 = (vq[0] + vq[1]) + (vq[2] + vq[3]);
    float var = vv2[0] + vv2[1];
    var += __shfl_xor(var, 16); var += __shfl_xor(var, 32);
    var = bfr(var * (1.0f / DM));
    const float den = bfr(sqrtf(bfr(var + 1e-5f)));
    const float inv = 1.0f / den;
    const float c0 = (mean - bfr(mean)) * inv;
    const f32x2 inv2 = f32x2{inv, inv}, c02 = f32x2{c0, c0};
    bf16x8 own[14];
#pragma unroll
    for (int i = 0; i < 14; ++i) {
      u32x4 o;
#pragma unroll
      for (int j = 0; j < 4; ++j) {
        f32x2 rr = d[i * 4 + j] * inv2 + c02;
        o[j] = pack2(rr[0], rr[1]);
      }
      own[i] = __builtin_bit_cast(bf16x8, o);
      *(u32x4*)(reg + i * 1024 + l * 16) = o;
    }
    STAMP(1);
    bar();
    const char* oth = smem + (2 * rg + (1 - cg)) * 14336 + l * 16;
#pragma unroll
    for (int i = 0; i < 14; ++i) {
      af[0][i] = own[i];
      af[1][i] = *(const bf16x8*)(oth + i * 1024);
    }
  }
  wait_vm<k1_cnt(TYPE, 1)>();
  asm volatile("" :: "v"(pf[0]), "v"(pf[1]), "v"(pf[2]), "v"(pf[3]));
  bar();
  issue(ic<2>{});
  issue(ic<3>{});
  STAMP(2);

  const int tl[2] = {mtile * 8 + rg * 2 + cg, mtile * 8 + rg * 2 + 1 - cg};
  const int f_ffn = q | ((g & 1) << 2);
  const int key2 = ((q >> 1) & 1) | ((g & 1) << 1);
  const int head = 2 * role + cg;
  int off1[4], off2[4], off0[6];
#pragma unroll
  for (int c = 0; c < 4; ++c) {
    int tile = (c >> 1) * 4 + 2 * cg + (c & 1);
    off1[c] = (8 * g + q) * 256 + ((tile ^ f_ffn) << 5) + 8 * p;
    off2[c] = (8 * g + q) * 128 + ((c ^ key2) << 5) + 8 * p;
  }
#pragma unroll
  for (int c = 0; c < 6; ++c) {
    int blk = c >> 1, ct = c & 1;
    int pi = 4 * ct + p + ((blk == 2 && head == 15) ? 1 : 0);
    pi = pi > 7 ? 7 : pi;
    int cc = 4 * cg + (pi >> 1);
    off0[c] = (8 * g + q) * 384 + blk * 128 + ((cc ^ (key2 << 1)) << 4) + 8 * (pi & 1);
  }

  bf16x8 bb[2][4];
  f32x4 acc[2][6];
  auto ldB = [&](auto tc, auto pc, bf16x8* dst) {
    constexpr int t = tc.value, ph = pc.value;
    constexpr int kind = k1_seg_kind(TYPE, t / 7);
    const char* slot = smem + k1_slot_off(t % 6);
    if constexpr (kind == 1) {
#pragma unroll
      for (int c = 0; c < 4; ++c) dst[c] = tr_frag(slot + ph * 8192 + off1[c], slot + ph * 8192 + 1024 + off1[c]);
    } else if constexpr (kind == 2) {
      if (cg == 0) {
#pragma unroll
        for (int c = 0; c < 4; ++c) dst[c] = tr_frag(slot + ph * 4096 + off2[c], slot + ph * 4096 + 512 + off2[c]);
      }
    } else {
      constexpr int ks = ph >> 1, c0 = 3 * (ph & 1);
#pragma unroll
      for (int c = 0; c < 3; ++c)
        dst[c] = tr_frag(slot + ks * 12288 + off0[c0 + c], slot + ks * 12288 + 1536 + off0[c0 + c]);
    }
  };
  auto mma = [&](auto tc, auto pc, const bf16x8* b) {
    constexpr int t = tc.value, ph = pc.value, s = t % 7;
    constexpr int kind = k1_seg_kind(TYPE, t / 7);
    const f32x4 z = f32x4{0.f, 0.f, 0.f, 0.f};
    if constexpr (kind == 1) {
      constexpr bool first = (s == 0 && ph == 0);
#pragma unroll
      for (int c = 0; c < 4; ++c) {
        acc[0][c] = mfma16(b[c], af[0][2 * s + ph], first ? z : acc[0][c]);
        acc[1][c] = mfma16(b[c], af[1][2 * s + ph], first ? z : acc[1][c]);
      }
    } else if constexpr (kind == 2) {
      constexpr bool first = (s == 0 && ph == 0);
      if (cg == 0) {
#pragma unroll
        for (int c = 0; c < 4; ++c) {
          acc[0][c] = mfma16(b[c], af[0][2 * s + ph], first ? z : acc[0][c]);
          acc[1][c] = mfma16(b[c], af[1][2 * s + ph], first ? z : acc[1][c]);
        }
      }
    } else {
      constexpr int ks = ph >> 1, c0 = 3 * (ph & 1);
      constexpr bool first = (s == 0 && ks == 0);
#pragma unroll
      for (int c = 0; c < 3; ++c) {
        if (c0 + c < 2) {
          acc[0][c0 + c] = mfma16(b[c], af[0][2 * s + ks], first ? z : acc[0][c0 + c]);
          acc[1][c0 + c] = mfma16(b[c], af[1][2 * s + ks], first ? z : acc[1][c0 + c]);
        } else {
          acc[0][c0 + c] = mfma16(af[0][2 * s + ks], b[c], first ? z : acc[0][c0 + c]);
          acc[1][c0 + c] = mfma16(af[1][2 * s + ks], b[c], first ? z : acc[1][c0 + c]);
        }
      }
    }
  };
  auto ffn_epi = [&](int kstep) {
#pragma unroll
    for (int rt = 0; rt < 2; ++rt) {
      float h[8];
#pragma unroll
      for (int c = 0; c < 2; ++c)
#pragma unroll
        for (int j = 0; j < 4; ++j) h[c * 4 + j] = silu_(acc[rt][c][j]) * acc[rt][2 + c][j];
      u32x4 o = {pack2(h[0], h[1]), pack2(h[2], h[3]), pack2(h[4], h[5]), pack2(h[6], h[7])};
      *(u32x4*)(ws + WS_H + ((size_t)(tl[rt] * 56 + kstep) * 64 + l) * 16) = o;
    }
  };

  ldB(ic<0>{}, ic<0>{}, bb[0]);

  static_for<0, NST>([&](auto tc) {
    constexpr int t = tc.value;
    constexpr int seg = t / 7, kind = k1_seg_kind(TYPE, seg);
    if constexpr (t % 2 == 0) {
      wait_vm<k1_cnt(TYPE, t + 3)>();
      bar();
    }
    STAMP(3 + t);
    constexpr int NPH = kind == 0 ? 4 : 2;
    static_for<0, NPH>([&](auto pc) {
      constexpr int ph = pc.value;
      if constexpr (ph > 0 || t % 2 == 1) __builtin_amdgcn_s_waitcnt(0xC07F);
      if constexpr (ph + 1 < NPH) ldB(tc, ic<ph + 1>{}, bb[(ph + 1) & 1]);
      else if constexpr (t + 1 < NST) ldB(ic<t + 1>{}, ic<0>{}, bb[0]);
      issue_piece(ic<t + 4>{}, pc);
      if constexpr (NPH == 2 && ph == 1) issue_piece(ic<t + 4>{}, ic<2>{});
      __builtin_amdgcn_sched_barrier(0);
      mma(tc, pc, bb[ph & 1]);
      __builtin_amdgcn_sched_barrier(0);
    });
    if constexpr (t % 7 == 6) {
      if constexpr (kind == 1) {
        ffn_epi((TYPE == 0 ? unit0 : unit0 + seg) * 2 + cg);
      } else if constexpr (kind == 2) {
        if (cg == 0) ffn_epi(48 + (role - 8));
      } else {
#pragma unroll
        for (int rt = 0; rt < 2; ++rt) {
          float v[8];
#pragma unroll
          for (int j = 0; j < 4; ++j) {
            v[j] = phi_(acc[rt][0][j]);
            float t1 = phi_(acc[rt][1][j]);
            v[4 + j] = g == 3 ? 0.f : t1;
          }
          u32x4 o = {pack2(v[0], v[1]), pack2(v[2], v[3]), pack2(v[4], v[5]), pack2(v[6], v[7])};
          *(u32x4*)(ws + WS_PQ + ((size_t)(tl[rt] * 16 + head) * 64 + l) * 16) = o;
        }
        bf16x8 pk[2], vv[2];
#pragma unroll
        for (int ct = 0; ct < 2; ++ct) {
          u32x4 a = {pack2(phi_(acc[0][2 + ct][0]), phi_(acc[0][2 + ct][1])),
                     pack2(phi_(acc[0][2 + ct][2]), phi_(acc[0][2 + ct][3])),
                     pack2(phi_(acc[1][2 + ct][0]), phi_(acc[1][2 + ct][1])),
                     pack2(phi_(acc[1][2 + ct][2]), phi_(acc[1][2 + ct][3]))};
          pk[ct] = __builtin_bit_cast(bf16x8, a);
          u32x4 b = {pack2(acc[0][4 + ct][0], acc[0][4 + ct][1]), pack2(acc[0][4 + ct][2], acc[0][4 + ct][3]),
                     pack2(acc[1][4 + ct][0], acc[1][4 + ct][1]), pack2(acc[1][4 + ct][2], acc[1][4 + ct][3])};
          if (ct == 1 && (l & 15) == 12) b = u32x4{0x3f803f80u, 0x3f803f80u, 0x3f803f80u, 0x3f803f80u};
          vv[ct] = __builtin_bit_cast(bf16x8, b);
        }
        f32x4 kvd[4];
#pragma unroll
        for (int dt = 0; dt < 2; ++dt)
#pragma unroll
          for (int et = 0; et < 2; ++et) kvd[dt * 2 + et] = mfma16(pk[dt], vv[et], f32x4{0.f, 0.f, 0.f, 0.f});
        bar();
        {
          char* sc = smem + k1_slot_off(w >> 1) + 16384 + (w & 1) * 4096;
#pragma unroll
          for (int i = 0; i < 4; ++i) *(f32x4*)(sc + i * 1024 + l * 16) = kvd[i];
        }
        bar();
        {
          const int hh = tid >> 8, idx = tid & 255;
          f32x4 sum = f32x4{0.f, 0.f, 0.f, 0.f};
#pragma unroll
          for (int r = 0; r < 4; ++r) {
            int ww = hh * 4 + r;
            sum += *(const f32x4*)(smem + k1_slot_off(ww >> 1) + 16384 + (ww & 1) * 4096 + idx * 16);
          }
          *(f32x4*)(ws + WS_KVP + ((size_t)(mtile * 16 + 2 * role + hh) * 256 + idx) * 16) = sum;
        }
      }
    }
  });
  STAMP(31);
}

__global__ __launch_bounds__(512, 2) void k1_kernel(const __hip_bfloat16* __restrict__ x,
                                                    const __hip_bfloat16* __restrict__ wqkv,
                                                    const __hip_bfloat16* __restrict__ w1,
                                                    const __hip_bfloat16* __restrict__ w3, char* ws,
                                                    const __hip_bfloat16* __restrict__ wo,
                                                    const __hip_bfloat16* __restrict__ w2) {
  __shared__ __attribute__((aligned(16))) char smem[163840];
  const int b = blockIdx.x;
  const int xc = b & 7, ix = b >> 3;
  const int role = (xc & 1) * 8 + (ix & 7), mtile = 4 * (xc >> 1) + (ix >> 3);
  if (b == 0 && threadIdx.x == 0) *(unsigned*)(ws + WS_FLAG) = 0u;
  if (role < 8) k1_body<0>(smem, x, wqkv, w1, w3, ws, mtile, role, wo, w2);
  else k1_body<1>(smem, x, wqkv, w1, w3, ws, mtile, role, wo, w2);
}

constexpr int K2_SLOT = 32768;
constexpr int K2_NSLOT = 4;
constexpr int K2_KV = K2_NSLOT * K2_SLOT;
constexpr int K2_NGEMM = 224;

constexpr int K2_PW = 10, K2_TF = 2;

__device__ __forceinline__ bool k2_flag_ready(unsigned lds0) {
  unsigned f;
  asm volatile("ds_read_b32 %0, %1\n\ts_waitcnt lgkmcnt(0)" : "=&v"(f) : "v"(lds0 + K2_KV + 31 * 1024) : "memory");
  return __builtin_amdgcn_readfirstlane(f) >= 16u;
}

__device__ __forceinline__ void k2_loader(char* smem, const __hip_bfloat16* __restrict__ wo,
                                          const __hip_bfloat16* __restrict__ w2, char* ws, int j, int mt, int n0) {
  const int l = threadIdx.x & 63;
  const int chs = (l & 7) ^ (((l >> 4) & 3) << 1);
  const char* src_w2 = (const char*)(w2 + (size_t)(32 * j + (l >> 3)) * DM + n0 + 8 * chs);
  const char* src_wo = (const char*)(wo + (size_t)(l >> 3) * DM + n0 + 8 * chs);
  const char* src_h = ws + WS_H + ((size_t)((mt * 4 + j) * 56) * 64 + l) * 16;
  const unsigned lds0 = (unsigned)(uintptr_t)(lds_void_ptr)smem;
  auto issue = [&](auto tc) {
    constexpr int t = tc.value;
    char* slot = smem + (t % K2_NSLOT) * K2_SLOT;
    if constexpr (t == K2_PW || t == K2_PW + 1) {
#pragma unroll
      for (int i = 0; i < 8; ++i) {
        int row0 = 224 * (t - K2_PW) + 8 * (8 * j + i);
        int row = row0 + (l >> 3);
        const char* sp = src_wo + (size_t)row0 * DM * 2;
        if (row > 447) sp -= (size_t)(row - 447) * DM * 2;
        glds16(sp, slot + (8 * j + i) * 1024);
      }
    } else if constexpr (t < 16) {
      constexpr int f = t < K2_PW ? t : t - 2;
#pragma unroll
      for (int i = 0; i < 4; ++i) glds16(src_w2 + (size_t)(f * 128 + 8 * i) * DM * 2, slot + (4 * j + i) * 1024);
#pragma unroll
      for (int i = 0; i < 4; ++i) glds16(src_h + (size_t)(f * 4 + i) * 1024, slot + 16384 + (j * 4 + i) * 1024);
    }
  };
  auto issue_kv = [&]() {
#pragma unroll
    for (int i = 0; i < 8; ++i)
      __builtin_amdgcn_global_load_lds((gbl_cvoid_ptr)(ws + WS_KVR + (size_t)((8 * j + i) * 64 + l) * 16),
                                       (lds_void_ptr)(smem + K2_KV + (8 * j + i) * 1024), 16, 0, 16);
  };
  static_for<0, K2_NSLOT - 1>([&](auto tc) { issue(tc); });
  bool kv_ready = false;
  static_for<0, 16>([&](auto tc) {
    constexpr int t = tc.value;
    wait_vm<8 * ((K2_NSLOT - 2) < (15 - t) ? (K2_NSLOT - 2) : (15 - t))>();
    bar();
    if constexpr (t == K2_PW) {
      if (!kv_ready) {
        unsigned spins = 0;
        while (__hip_atomic_load((unsigned*)(ws + WS_FLAG), __ATOMIC_RELAXED, __HIP_MEMORY_SCOPE_AGENT) < 16u) {
          __builtin_amdgcn_s_sleep(2);
          if (++spins > (1u << 24)) break;
        }
        asm volatile("" ::: "memory");
        issue_kv();
        wait_vm<0>();
        bar();
      }
    }
    issue(ic<t + K2_NSLOT - 1>{});
    if constexpr (t == K2_TF) {
      if (j == 0)
        __builtin_amdgcn_global_load_lds((gbl_cvoid_ptr)(ws + WS_FLAG), (lds_void_ptr)(smem + K2_KV + 31 * 1024), 4, 0, 16);
    }
    if constexpr (t == K2_TF + 3) kv_ready = k2_flag_ready(lds0);
    if constexpr (t == K2_TF + 4) {
      if (kv_ready) issue_kv();
    }
  });
  bar();
}

__device__ __forceinline__ void k2_gemm(char* smem, const __hip_bfloat16* __restrict__ x, char* ws,
                                        __hip_bfloat16* __restrict__ out, int w, int mt, int n0) {
  const int tid = threadIdx.x, l = tid & 63;
  const int rt = w & 3, kg = w >> 2;
  const int g = l >> 4, q = (l & 15) >> 2, p = l & 3;
  constexpr int STAMP_K = 1; STAMP(0);

  u32x4 pqf[8];
#pragma unroll
  for (int hh = 0; hh < 8; ++hh)
    pqf[hh] = *(const u32x4*)(ws + WS_PQ + ((size_t)((mt * 4 + rt) * 16 + 2 * hh + kg) * 64 + l) * 16);
  const int orow = mt * 64 + rt * 16 + (l & 15);
  u32x2 xr[2];
#pragma unroll
  for (int i = 0; i < 2; ++i) xr[i] = *(const u32x2*)(x + (size_t)orow * DM + n0 + (2 * kg + i) * 16 + 4 * g);
  asm volatile("" ::: "memory");

  f32x4 acc1[4], acc2[4];
#pragma unroll
  for (int c = 0; c < 4; ++c) { acc1[c] = f32x4{0.f, 0.f, 0.f, 0.f}; acc2[c] = acc1[c]; }

  int offa[4], offwa[4], offwb[4];
#pragma unroll
  for (int c = 0; c < 4; ++c) {
    int r1 = 4 * g + q;
    offa[c] = r1 * 128 + ((c ^ ((r1 >> 1) & 3)) << 5) + 8 * p;
    int r2 = 16 + 4 * (g > 2 ? 2 : g) + q;
    offwa[c] = (28 * kg + r1) * 128 + ((c ^ ((2 * kg + (r1 >> 1)) & 3)) << 5) + 8 * p;
    offwb[c] = (28 * kg + r2) * 128 + ((c ^ ((2 * kg + (r2 >> 1)) & 3)) << 5) + 8 * p;
  }
  bool kv_ready = false;
  const unsigned lds0 = (unsigned)(uintptr_t)(lds_void_ptr)smem;
  bf16x8 of[8];

  static_for<0, 16>([&](auto tc) {
    constexpr int t = tc.value;
    bar(); STAMP(1 + t);
    const char* slot = smem + (t % K2_NSLOT) * K2_SLOT;
    if constexpr (t == K2_PW || t == K2_PW + 1) {
      if constexpr (t == K2_PW) {
        if (!kv_ready) bar();
        f32x4 n0v[8], n1v[8];
#pragma unroll
        for (int hh = 0; hh < 8; ++hh) {
          bf16x8 pq = __builtin_bit_cast(bf16x8, pqf[hh]);
          const char* kp = smem + K2_KV + (2 * hh + kg) * 2048 + l * 16;
          n0v[hh] = mfma16(*(const bf16x8*)kp, pq, f32x4{0.f, 0.f, 0.f, 0.f});
          n1v[hh] = mfma16(*(const bf16x8*)(kp + 1024), pq, f32x4{0.f, 0.f, 0.f, 0.f});
        }
        float den[8];
#pragma unroll
        for (int hh = 0; hh < 8; ++hh) den[hh] = __shfl(n1v[hh][0], (l & 15) + 48);
#pragma unroll
        for (int hh = 0; hh < 8; ++hh) {
          const float inv = rcp_(den[hh] + 1e-6f);
          const float invm = g == 3 ? 0.f : inv;
          u32x4 ov = {pack2(n0v[hh][0] * inv, n0v[hh][1] * inv), pack2(n0v[hh][2] * inv, n0v[hh][3] * inv),
                      pack2(n1v[hh][0] * invm, n1v[hh][1] * invm), pack2(n1v[hh][2] * invm, n1v[hh][3] * invm)};
          of[hh] = __builtin_bit_cast(bf16x8, ov);
        }
      }
#pragma unroll
      for (int i = 0; i < 4; ++i) {
        const int hh = (t - K2_PW) * 4 + i;
#pragma unroll
        for (int c = 0; c < 4; ++c) {
          bf16x8 bfg = tr_frag(slot + i * 7168 + offwa[c], slot + i * 7168 + offwb[c]);
          acc2[c] = mfma16(bfg, of[hh], acc2[c]);
        }
      }
    } else {
      const bf16x8 hf0 = *(const bf16x8*)(slot + 16384 + (rt * 4 + kg) * 1024 + l * 16);
      const bf16x8 hf1 = *(const bf16x8*)(slot + 16384 + (rt * 4 + kg + 2) * 1024 + l * 16);
      bf16x8 bA[4], bB[4];
#pragma unroll
      for (int c = 0; c < 4; ++c) bA[c] = tr_frag(slot + kg * 4096 + offa[c], slot + kg * 4096 + 2048 + offa[c]);
#pragma unroll
      for (int c = 0; c < 4; ++c) bB[c] = tr_frag(slot + (kg + 2) * 4096 + offa[c], slot + (kg + 2) * 4096 + 2048 + offa[c]);
#pragma unroll
      for (int c = 0; c < 4; ++c) acc1[c] = mfma16(bA[c], hf0, acc1[c]);
#pragma unroll
      for (int c = 0; c < 4; ++c) acc1[c] = mfma16(bB[c], hf1, acc1[c]);
    }
    if constexpr (t == K2_TF + 3) kv_ready = k2_flag_ready(lds0);
  });

  STAMP(17);
  {
    char* ex = smem + K2_SLOT + w * 4096;
    *(f32x4*)(ex + 0 * 1024 + l * 16) = kg ? acc1[0] : acc1[2];
    *(f32x4*)(ex + 1 * 1024 + l * 16) = kg ? acc1[1] : acc1[3];
    *(f32x4*)(ex + 2 * 1024 + l * 16) = kg ? acc2[0] : acc2[2];
    *(f32x4*)(ex + 3 * 1024 + l * 16) = kg ? acc2[1] : acc2[3];
  }
  bar();
  {
    const char* ex = smem + K2_SLOT + (w ^ 4) * 4096;
#pragma unroll
    for (int i = 0; i < 2; ++i) {
      f32x4 a1 = (kg ? acc1[2 + i] : acc1[i]) + *(const f32x4*)(ex + i * 1024 + l * 16);
      f32x4 a2 = (kg ? acc2[2 + i] : acc2[i]) + *(const f32x4*)(ex + (2 + i) * 1024 + l * 16);
      const int col = n0 + (2 * kg + i) * 16 + 4 * g;
      float xv[4] = {bf_lo(xr[i][0]), bf_hi(xr[i][0]), bf_lo(xr[i][1]), bf_hi(xr[i][1])};
      float r[4];
#pragma unroll
      for (int jj = 0; jj < 4; ++jj) r[jj] = xv[jj] + bfr(a2[jj]) * bfr(silu_(bfr(a1[jj])));
      u32x2 ov = {pack2(r[0], r[1]), pack2(r[2], r[3])};
      *(u32x2*)(out + (size_t)orow * DM + col) = ov;
    }
  }
  STAMP(18);
}

__global__ __launch_bounds__(768, 3) void k2_kernel(const __hip_bfloat16* __restrict__ x,
                                                    const __hip_bfloat16* __restrict__ wo,
                                                    const __hip_bfloat16* __restrict__ w2,
                                                    char* ws, __hip_bfloat16* __restrict__ out) {
  __shared__ __attribute__((aligned(16))) char smem[K2_NSLOT * K2_SLOT + 32768];
  const int tid = threadIdx.x, l = tid & 63;
  const int b = blockIdx.x;
  if (b >= K2_NGEMM) {
    const int head = b - K2_NGEMM;
    if (tid < 128) {
      const int et = tid >> 6;
      f32x4 s0 = f32x4{0.f, 0.f, 0.f, 0.f}, s1 = s0;
#pragma unroll
      for (int m = 0; m < 16; ++m) {
        const char* pb = ws + WS_KVP + ((size_t)(m * 16 + head) * 256) * 16;
        s0 += *(const f32x4*)(pb + ((0 * 2 + et) * 64 + l) * 16);
        s1 += *(const f32x4*)(pb + ((1 * 2 + et) * 64 + l) * 16);
      }
      if ((l >> 4) == 3) s1 = f32x4{0.f, 0.f, 0.f, 0.f};
      if (et == 1 && (l & 15) > 12) { s0 = f32x4{0.f, 0.f, 0.f, 0.f}; s1 = s0; }
      u32x4 o = {pack2(s0[0], s0[1]), pack2(s0[2], s0[3]), pack2(s1[0], s1[1]), pack2(s1[2], s1[3])};
      const __amdgpu_buffer_rsrc_t rs = __builtin_amdgcn_make_buffer_rsrc(ws + WS_KVR, 0, 32768, 0x00020000);
      __builtin_amdgcn_raw_buffer_store_b128(o, rs, (unsigned)(((head * 2 + et) * 64 + l) * 16), 0, 16);
      asm volatile("s_waitcnt vmcnt(0)" ::: "memory");
    }
    __syncthreads();
    if (tid == 0)
      __hip_atomic_fetch_add((unsigned*)(ws + WS_FLAG), 1u, __ATOMIC_RELAXED, __HIP_MEMORY_SCOPE_AGENT);
    return;
  }
  const int xcd = b & 7, idx = b >> 3;
  const int mt = 4 * xcd + idx / 7, n0 = (idx % 7) * 64;
  const int w = __builtin_amdgcn_readfirstlane(tid >> 6);
  if (w >= 8) k2_loader(smem, wo, w2, ws, w - 8, mt, n0);
  else k2_gemm(smem, x, ws, out, w, mt, n0);
}

extern "C" void kernel_launch(void* const* d_in, const int* in_sizes, int n_in, void* d_out, int out_size,
                              void* d_ws, size_t ws_size, hipStream_t stream) {
  const __hip_bfloat16* x = (const __hip_bfloat16*)d_in[0];
  const __hip_bfloat16* wqkv = (const __hip_bfloat16*)d_in[1];
  const __hip_bfloat16* wo = (const __hip_bfloat16*)d_in[2];
  const __hip_bfloat16* w1 = (const __hip_bfloat16*)d_in[4];
  const __hip_bfloat16* w3 = (const __hip_bfloat16*)d_in[5];
  const __hip_bfloat16* w2 = (const __hip_bfloat16*)d_in[6];
  char* ws = (char*)d_ws;
  k1_kernel<<<256, 512, 0, stream>>>(x, wqkv, w1, w3, ws, wo, w2);
  k2_kernel<<<K2_NGEMM + 16, 768, 0, stream>>>(x, wo, w2, ws, (__hip_bfloat16*)d_out);
}
```

```cpp
#include <hip/hip_runtime.h>
#include <hip/hip_bf16.h>
#include <stdint.h>
#include <type_traits>

typedef __attribute__((ext_vector_type(8))) __bf16 bf16x8;
typedef __attribute__((ext_vector_type(4))) short s16x4;
typedef __attribute__((ext_vector_type(8))) short s16x8;
typedef __attribute__((ext_vector_type(4))) float f32x4;
typedef __attribute__((ext_vector_type(2))) float f32x2;
typedef __attribute__((ext_vector_type(4))) unsigned u32x4;
typedef __attribute__((ext_vector_type(2))) unsigned u32x2;
typedef __attribute__((address_space(3))) s16x4* lds_s16x4_ptr;
typedef __attribute__((address_space(3))) void* lds_void_ptr;
typedef const __attribute__((address_space(1))) void* gbl_cvoid_ptr;

constexpr int SEQ = 2048, DM = 448, FF = 1792, NH = 16, DH = 28;

constexpr size_t WS_H    = 0;
constexpr size_t WS_PQ   = 7340032;
constexpr size_t WS_KVP  = WS_PQ + 2097152;
constexpr size_t WS_KVR  = WS_KVP + 1048576;
constexpr size_t WS_FLAG = WS_KVR + 32768;


template <int I, int N, class Fn>
__device__ __forceinline__ void static_for(Fn&& f) {
  if constexpr (I < N) { f(std::integral_constant<int, I>{}); static_for<I + 1, N>(f); }
}
template <int N> using ic = std::integral_constant<int, N>;
template <int N> __device__ __forceinline__ void wait_vm() {
  asm volatile("" ::: "memory");
  __builtin_amdgcn_s_waitcnt((N & 0xF) | (7 << 4) | (0xF << 8) | ((N >> 4) << 14));
  asm volatile("" ::: "memory");
}
__device__ __forceinline__ void bar() {
  asm volatile("" ::: "memory");
  __builtin_amdgcn_s_waitcnt(0xC07F);
  __builtin_amdgcn_s_barrier();
  asm volatile("" ::: "memory");
}

__device__ __forceinline__ float bfr(float f) { return (float)(__bf16)f; }
__device__ __forceinline__ float bf_lo(unsigned u) { return __uint_as_float(u << 16); }
__device__ __forceinline__ float bf_hi(unsigned u) { return __uint_as_float(u & 0xffff0000u); }
__device__ __forceinline__ unsigned pack2(float a, float b) {
  typedef __attribute__((ext_vector_type(2))) __bf16 bf16x2;
  bf16x2 v; v[0] = (__bf16)a; v[1] = (__bf16)b;
  return __builtin_bit_cast(unsigned, v);
}
__device__ __forceinline__ void glds16(const void* g, void* l) {
  __builtin_amdgcn_global_load_lds((gbl_cvoid_ptr)g, (lds_void_ptr)l, 16, 0, 0);
}
__device__ __forceinline__ bf16x8 tr_frag(const char* lds_a, const char* lds_b) {
  s16x4 lo = __builtin_amdgcn_ds_read_tr16_b64_v4i16((lds_s16x4_ptr)lds_a);
  s16x4 hi = __builtin_amdgcn_ds_read_tr16_b64_v4i16((lds_s16x4_ptr)lds_b);
  s16x8 v = __builtin_shufflevector(lo, hi, 0, 1, 2, 3, 4, 5, 6, 7);
  return __builtin_bit_cast(bf16x8, v);
}
__device__ __forceinline__ f32x4 mfma16(bf16x8 a, bf16x8 b, f32x4 c) {
  return __builtin_amdgcn_mfma_f32_16x16x32_bf16(a, b, c, 0, 0, 0);
}
__device__ __forceinline__ float rcp_(float v) { return __builtin_amdgcn_rcpf(v); }
__device__ __forceinline__ float exp_(float v) { return __builtin_amdgcn_exp2f(v * 1.44269504088896f); }
__device__ __forceinline__ float silu_(float v) { return v * rcp_(1.0f + exp_(-v)); }
__device__ __forceinline__ float phi_(float t) { return (t > 0.0f ? t + 1.0f : exp_(t)) + 1e-6f; }

constexpr int K1_SLOT = 24576;
constexpr int K1_XN_BYTES = 114688;
__host__ __device__ constexpr int k1_slot_off(int s) { return s < 2 ? K1_XN_BYTES + K1_SLOT * s : K1_SLOT * (s - 2); }
__host__ __device__ constexpr int k1_seg_kind(int type, int seg) {
  return type == 0 ? (seg == 0 ? 0 : 1) : (seg < 2 ? 1 : 2);
}
__host__ __device__ constexpr int k1_nstage(int type) { return type == 0 ? 14 : 21; }
__host__ __device__ constexpr int k1_cnt(int type, int t) {
  return t >= k1_nstage(type) ? 0 : (k1_seg_kind(type, t / 7) == 0 ? 3 : (k1_seg_kind(type, t / 7) == 1 ? 2 : 1));
}

template <int TYPE>
__device__ __forceinline__ void k1_body(char* smem, const __hip_bfloat16* __restrict__ x,
                                        const __hip_bfloat16* __restrict__ wqkv,
                                        const __hip_bfloat16* __restrict__ w1,
                                        const __hip_bfloat16* __restrict__ w3, char* ws, int mtile,
                                        int role, const __hip_bfloat16* __restrict__ wo,
                                        const __hip_bfloat16* __restrict__ w2) {
  const int tid = threadIdx.x, w = __builtin_amdgcn_readfirstlane(tid >> 6), l = tid & 63;
  const int rg = w & 3, cg = w >> 2;
  const int g = l >> 4, q = (l & 15) >> 2, p = l & 3;
  constexpr int NSEG = TYPE == 0 ? 2 : 3;
  constexpr int NST = k1_nstage(TYPE);

  const char* src[NSEG][3];
  const int unit0 = TYPE == 0 ? role : 8 + 2 * (role - 8);
  static_for<0, NSEG>([&](auto sc) {
    constexpr int seg = sc.value;
    constexpr int kind = k1_seg_kind(TYPE, seg);
    if constexpr (kind == 1) {
      int n0 = (TYPE == 0 ? unit0 : unit0 + seg) * 64;
      int f = (l >> 4) | ((w & 1) << 2);
      int ch = (l & 15) ^ (f << 1);
      const __hip_bfloat16* base = ch < 8 ? w1 : w3;
      for (int b = 0; b < 2; ++b) {
        int row = 8 * w + 4 * b + (l >> 4);
        src[seg][b] = (const char*)(base + (size_t)row * FF + n0 + 8 * (ch & 7));
      }
    } else if constexpr (kind == 2) {
      int n0 = 1536 + 32 * (role - 8);
      int rr = l >> 3;
      int key = ((rr >> 1) & 1) | ((w & 1) << 1);
      int ch = (l & 7) ^ (key << 1);
      const __hip_bfloat16* base = ch < 4 ? w1 : w3;
      int row = 8 * w + rr;
      src[seg][0] = (const char*)(base + (size_t)row * FF + n0 + 8 * (ch & 3));
    } else {
      for (int b = 0; b < 3; ++b) {
        int cl = 64 * (3 * w + b) + l;
        int row = cl / 24, chp = cl % 24;
        int blk = chp >> 3;
        int key = ((row >> 1) & 1) | (((row >> 3) & 1) << 1);
        int cc = (chp & 7) ^ (key << 1);
        int head = 2 * role + (cc >> 2);
        int col = blk * DM + DH * head + 8 * (cc & 3);
        if (blk == 2 && head == 15) col -= 4;
        src[seg][b] = (const char*)(wqkv + (size_t)row * (3 * DM) + col);
      }
    }
  });

  auto issue = [&](auto tc) {
    constexpr int t = tc.value;
    if constexpr (t < NST) {
      constexpr int seg = t / 7, s = t % 7, kind = k1_seg_kind(TYPE, seg);
      char* slot = smem + k1_slot_off(t % 6);
      if constexpr (kind == 1) {
        glds16(src[seg][0] + (size_t)s * 64 * FF * 2, slot + (8 * w) * 256);
        glds16(src[seg][1] + (size_t)s * 64 * FF * 2, slot + (8 * w + 4) * 256);
      } else if constexpr (kind == 2) {
        glds16(src[seg][0] + (size_t)s * 64 * FF * 2, slot + w * 1024);
      } else {
        glds16(src[seg][0] + (size_t)s * 64 * (3 * DM) * 2, slot + (3 * w) * 1024);
        glds16(src[seg][1] + (size_t)s * 64 * (3 * DM) * 2, slot + (3 * w + 1) * 1024);
        glds16(src[seg][2] + (size_t)s * 64 * (3 * DM) * 2, slot + (3 * w + 2) * 1024);
      }
    }
  };

  auto issue_piece = [&](auto tc, auto ic_) {
    constexpr int t = tc.value, i = ic_.value;
    if constexpr (t < NST && i < k1_cnt(TYPE, t)) {
      constexpr int seg = t / 7, s = t % 7, kind = k1_seg_kind(TYPE, seg);
      char* slot = smem + k1_slot_off(t % 6);
      if constexpr (kind == 1) glds16(src[seg][i] + (size_t)s * 64 * FF * 2, slot + (8 * w + 4 * i) * 256);
      else if constexpr (kind == 2) glds16(src[seg][0] + (size_t)s * 64 * FF * 2, slot + w * 1024);
      else glds16(src[seg][i] + (size_t)s * 64 * (3 * DM) * 2, slot + (3 * w + i) * 1024);
    }
  };

  bf16x8 af[2][14];
  unsigned pf[4];
  {
    const int tile = 2 * rg + cg;
    char* reg = smem + tile * 14336;
    const char* xb = (const char*)(x + (size_t)(mtile * 128 + 16 * tile) * DM);
#pragma unroll
    for (int i = 0; i < 14; ++i) {
      const int cl = 64 * i + l, row = cl / 56, chp = cl % 56;
      glds16(xb + row * 896 + ((chp ^ (row & 7)) << 4), reg + i * 1024);
    }
    {
      const int wid = blockIdx.x * 8 + w;
#pragma unroll
      for (int k = 0; k < 4; ++k) {
        int pc = wid + 2048 * k;
        pc = pc < 6272 ? pc : 6271;
        const char* bp = pc < 1568 ? (const char*)w1 + (size_t)pc * 1024
                       : pc < 3136 ? (const char*)w3 + (size_t)(pc - 1568) * 1024
                       : pc < 4312 ? (const char*)wqkv + (size_t)(pc - 3136) * 1024
                       : pc < 5880 ? (const char*)w2 + (size_t)(pc - 4312) * 1024
                                   : (const char*)wo + (size_t)(pc - 5880) * 1024;
        asm volatile("global_load_dword %0, %1, off" : "=v"(pf[k]) : "v"(bp + l * 16) : "memory");
      }
    }
    wait_vm<4>();
    issue(ic<0>{});
    issue(ic<1>{});
    const int r = l & 15;
    const unsigned ra = (unsigned)(uintptr_t)(lds_void_ptr)reg + r * 896;
    const unsigned a0 = ra + ((g ^ (r & 7)) << 4), a1 = ra + (((4 + g) ^ (r & 7)) << 4);
    u32x4 raw[14];
    asm volatile(
        "ds_read_b128 %0, %14\n\tds_read_b128 %1, %15\n\t"
        "ds_read_b128 %2, %14 offset:128\n\tds_read_b128 %3, %15 offset:128\n\t"
        "ds_read_b128 %4, %14 offset:256\n\tds_read_b128 %5, %15 offset:256\n\t"
        "ds_read_b128 %6, %14 offset:384\n\tds_read_b128 %7, %15 offset:384\n\t"
        "ds_read_b128 %8, %14 offset:512\n\tds_read_b128 %9, %15 offset:512\n\t"
        "ds_read_b128 %10, %14 offset:640\n\tds_read_b128 %11, %15 offset:640\n\t"
        "ds_read_b128 %12, %14 offset:768\n\tds_read_b128 %13, %15 offset:768\n\t"
        "s_waitcnt lgkmcnt(0)"
        : "=&v"(raw[0]), "=&v"(raw[1]), "=&v"(raw[2]), "=&v"(raw[3]), "=&v"(raw[4]), "=&v"(raw[5]), "=&v"(raw[6]),
          "=&v"(raw[7]), "=&v"(raw[8]), "=&v"(raw[9]), "=&v"(raw[10]), "=&v"(raw[11]), "=&v"(raw[12]), "=&v"(raw[13])
        : "v"(a0), "v"(a1) : "memory");

    f32x2 d[56];
    f32x2 sv[4] = {f32x2{0.f, 0.f}, f32x2{0.f, 0.f}, f32x2{0.f, 0.f}, f32x2{0.f, 0.f}};
#pragma unroll
    for (int i = 0; i < 14; ++i)
#pragma unroll
      for (int j = 0; j < 4; ++j) {
        d[i * 4 + j] = f32x2{bf_lo(raw[i][j]), bf_hi(raw[i][j])};
        sv[j] += d[i * 4 + j];
      }
    f32x2 svs = (sv[0] + sv[1]) + (sv[2] + sv[3]);
    float sum = svs[0] + svs[1];
    sum += __shfl_xor(sum, 16); sum += __shfl_xor(sum, 32);
    const float mean = sum * (1.0f / DM);
    const f32x2 mean2 = f32x2{mean, mean};
    f32x2 vq[4] = {f32x2{0.f, 0.f}, f32x2{0.f, 0.f}, f32x2{0.f, 0.f}, f32x2{0.f, 0.f}};
#pragma unroll
    for (int i = 0; i < 56; ++i) { d[i] -= mean2; vq[i & 3] += d[i] * d[i]; }
    f32x2 vv2 = (vq[0] + vq[1]) + (vq[2] + vq[3]);
    float var = vv2[0] + vv2[1];
    var += __shfl_xor(var, 16); var += __shfl_xor(var, 32);
    var = bfr(var * (1.0f / DM));
    const float den = bfr(sqrtf(bfr(var + 1e-5f)));
    const float inv = 1.0f / den;
    const float c0 = (mean - bfr(mean)) * inv;
    const f32x2 inv2 = f32x2{inv, inv}, c02 = f32x2{c0, c0};
    bf16x8 own[14];
#pragma unroll
    for (int i = 0; i < 14; ++i) {
      u32x4 o;
#pragma unroll
      for (int j = 0; j < 4; ++j) {
        f32x2 rr = d[i * 4 + j] * inv2 + c02;
        o[j] = pack2(rr[0], rr[1]);
      }
      own[i] = __builtin_bit_cast(bf16x8, o);
      *(u32x4*)(reg + i * 1024 + l * 16) = o;
    }

    bar();
    const char* oth = smem + (2 * rg + (1 - cg)) * 14336 + l * 16;
#pragma unroll
    for (int i = 0; i < 14; ++i) {
      af[0][i] = own[i];
      af[1][i] = *(const bf16x8*)(oth + i * 1024);
    }
  }
  wait_vm<k1_cnt(TYPE, 1)>();
  asm volatile("" :: "v"(pf[0]), "v"(pf[1]), "v"(pf[2]), "v"(pf[3]));
  bar();
  issue(ic<2>{});
  issue(ic<3>{});


  const int tl[2] = {mtile * 8 + rg * 2 + cg, mtile * 8 + rg * 2 + 1 - cg};
  const int f_ffn = q | ((g & 1) << 2);
  const int key2 = ((q >> 1) & 1) | ((g & 1) << 1);
  const int head = 2 * role + cg;
  int off1[4], off2[4], off0[6];
#pragma unroll
  for (int c = 0; c < 4; ++c) {
    int tile = (c >> 1) * 4 + 2 * cg + (c & 1);
    off1[c] = (8 * g + q) * 256 + ((tile ^ f_ffn) << 5) + 8 * p;
    off2[c] = (8 * g + q) * 128 + ((c ^ key2) << 5) + 8 * p;
  }
#pragma unroll
  for (int c = 0; c < 6; ++c) {
    int blk = c >> 1, ct = c & 1;
    int pi = 4 * ct + p + ((blk == 2 && head == 15) ? 1 : 0);
    pi = pi > 7 ? 7 : pi;
    int cc = 4 * cg + (pi >> 1);
    off0[c] = (8 * g + q) * 384 + blk * 128 + ((cc ^ (key2 << 1)) << 4) + 8 * (pi & 1);
  }

  bf16x8 bb[2][4];
  f32x4 acc[2][6];
  auto ldB = [&](auto tc, auto pc, bf16x8* dst) {
    constexpr int t = tc.value, ph = pc.value;
    constexpr int kind = k1_seg_kind(TYPE, t / 7);
    const char* slot = smem + k1_slot_off(t % 6);
    if constexpr (kind == 1) {
#pragma unroll
      for (int c = 0; c < 4; ++c) dst[c] = tr_frag(slot + ph * 8192 + off1[c], slot + ph * 8192 + 1024 + off1[c]);
    } else if constexpr (kind == 2) {
      if (cg == 0) {
#pragma unroll
        for (int c = 0; c < 4; ++c) dst[c] = tr_frag(slot + ph * 4096 + off2[c], slot + ph * 4096 + 512 + off2[c]);
      }
    } else {
      constexpr int ks = ph >> 1, c0 = 3 * (ph & 1);
#pragma unroll
      for (int c = 0; c < 3; ++c)
        dst[c] = tr_frag(slot + ks * 12288 + off0[c0 + c], slot + ks * 12288 + 1536 + off0[c0 + c]);
    }
  };
  auto mma = [&](auto tc, auto pc, const bf16x8* b) {
    constexpr int t = tc.value, ph = pc.value, s = t % 7;
    constexpr int kind = k1_seg_kind(TYPE, t / 7);
    const f32x4 z = f32x4{0.f, 0.f, 0.f, 0.f};
    if constexpr (kind == 1) {
      constexpr bool first = (s == 0 && ph == 0);
#pragma unroll
      for (int c = 0; c < 4; ++c) {
        acc[0][c] = mfma16(b[c], af[0][2 * s + ph], first ? z : acc[0][c]);
        acc[1][c] = mfma16(b[c], af[1][2 * s + ph], first ? z : acc[1][c]);
      }
    } else if constexpr (kind == 2) {
      constexpr bool first = (s == 0 && ph == 0);
      if (cg == 0) {
#pragma unroll
        for (int c = 0; c < 4; ++c) {
          acc[0][c] = mfma16(b[c], af[0][2 * s + ph], first ? z : acc[0][c]);
          acc[1][c] = mfma16(b[c], af[1][2 * s + ph], first ? z : acc[1][c]);
        }
      }
    } else {
      constexpr int ks = ph >> 1, c0 = 3 * (ph & 1);
      constexpr bool first = (s == 0 && ks == 0);
#pragma unroll
      for (int c = 0; c < 3; ++c) {
        if (c0 + c < 2) {
          acc[0][c0 + c] = mfma16(b[c], af[0][2 * s + ks], first ? z : acc[0][c0 + c]);
          acc[1][c0 + c] = mfma16(b[c], af[1][2 * s + ks], first ? z : acc[1][c0 + c]);
        } else {
          acc[0][c0 + c] = mfma16(af[0][2 * s + ks], b[c], first ? z : acc[0][c0 + c]);
          acc[1][c0 + c] = mfma16(af[1][2 * s + ks], b[c], first ? z : acc[1][c0 + c]);
        }
      }
    }
  };
  auto ffn_epi = [&](int kstep) {
#pragma unroll
    for (int rt = 0; rt < 2; ++rt) {
      float h[8];
#pragma unroll
      for (int c = 0; c < 2; ++c)
#pragma unroll
        for (int j = 0; j < 4; ++j) h[c * 4 + j] = silu_(acc[rt][c][j]) * acc[rt][2 + c][j];
      u32x4 o = {pack2(h[0], h[1]), pack2(h[2], h[3]), pack2(h[4], h[5]), pack2(h[6], h[7])};
      *(u32x4*)(ws + WS_H + ((size_t)(tl[rt] * 56 + kstep) * 64 + l) * 16) = o;
    }
  };

  ldB(ic<0>{}, ic<0>{}, bb[0]);

  static_for<0, NST>([&](auto tc) {
    constexpr int t = tc.value;
    constexpr int seg = t / 7, kind = k1_seg_kind(TYPE, seg);
    if constexpr (t % 2 == 0) {
      wait_vm<k1_cnt(TYPE, t + 3)>();
      bar();
    }

    constexpr int NPH = kind == 0 ? 4 : 2;
    static_for<0, NPH>([&](auto pc) {
      constexpr int ph = pc.value;
      if constexpr (ph > 0 || t % 2 == 1) __builtin_amdgcn_s_waitcnt(0xC07F);
      if constexpr (ph + 1 < NPH) ldB(tc, ic<ph + 1>{}, bb[(ph + 1) & 1]);
      else if constexpr (t + 1 < NST) ldB(ic<t + 1>{}, ic<0>{}, bb[0]);
      issue_piece(ic<t + 4>{}, pc);
      if constexpr (NPH == 2 && ph == 1) issue_piece(ic<t + 4>{}, ic<2>{});
      __builtin_amdgcn_sched_barrier(0);
      mma(tc, pc, bb[ph & 1]);
      __builtin_amdgcn_sched_barrier(0);
    });
    if constexpr (t % 7 == 6) {
      if constexpr (kind == 1) {
        ffn_epi((TYPE == 0 ? unit0 : unit0 + seg) * 2 + cg);
      } else if constexpr (kind == 2) {
        if (cg == 0) ffn_epi(48 + (role - 8));
      } else {
#pragma unroll
        for (int rt = 0; rt < 2; ++rt) {
          float v[8];
#pragma unroll
          for (int j = 0; j < 4; ++j) {
            v[j] = phi_(acc[rt][0][j]);
            float t1 = phi_(acc[rt][1][j]);
            v[4 + j] = g == 3 ? 0.f : t1;
          }
          u32x4 o = {pack2(v[0], v[1]), pack2(v[2], v[3]), pack2(v[4], v[5]), pack2(v[6], v[7])};
          *(u32x4*)(ws + WS_PQ + ((size_t)(tl[rt] * 16 + head) * 64 + l) * 16) = o;
        }
        bf16x8 pk[2], vv[2];
#pragma unroll
        for (int ct = 0; ct < 2; ++ct) {
          u32x4 a = {pack2(phi_(acc[0][2 + ct][0]), phi_(acc[0][2 + ct][1])),
                     pack2(phi_(acc[0][2 + ct][2]), phi_(acc[0][2 + ct][3])),
                     pack2(phi_(acc[1][2 + ct][0]), phi_(acc[1][2 + ct][1])),
                     pack2(phi_(acc[1][2 + ct][2]), phi_(acc[1][2 + ct][3]))};
          pk[ct] = __builtin_bit_cast(bf16x8, a);
          u32x4 b = {pack2(acc[0][4 + ct][0], acc[0][4 + ct][1]), pack2(acc[0][4 + ct][2], acc[0][4 + ct][3]),
                     pack2(acc[1][4 + ct][0], acc[1][4 + ct][1]), pack2(acc[1][4 + ct][2], acc[1][4 + ct][3])};
          if (ct == 1 && (l & 15) == 12) b = u32x4{0x3f803f80u, 0x3f803f80u, 0x3f803f80u, 0x3f803f80u};
          vv[ct] = __builtin_bit_cast(bf16x8, b);
        }
        f32x4 kvd[4];
#pragma unroll
        for (int dt = 0; dt < 2; ++dt)
#pragma unroll
          for (int et = 0; et < 2; ++et) kvd[dt * 2 + et] = mfma16(pk[dt], vv[et], f32x4{0.f, 0.f, 0.f, 0.f});
        bar();
        {
          char* sc = smem + k1_slot_off(w >> 1) + 16384 + (w & 1) * 4096;
#pragma unroll
          for (int i = 0; i < 4; ++i) *(f32x4*)(sc + i * 1024 + l * 16) = kvd[i];
        }
        bar();
        {
          const int hh = tid >> 8, idx = tid & 255;
          f32x4 sum = f32x4{0.f, 0.f, 0.f, 0.f};
#pragma unroll
          for (int r = 0; r < 4; ++r) {
            int ww = hh * 4 + r;
            sum += *(const f32x4*)(smem + k1_slot_off(ww >> 1) + 16384 + (ww & 1) * 4096 + idx * 16);
          }
          *(f32x4*)(ws + WS_KVP + ((size_t)(mtile * 16 + 2 * role + hh) * 256 + idx) * 16) = sum;
        }
      }
    }
  });

}

__global__ __launch_bounds__(512, 2) void k1_kernel(const __hip_bfloat16* __restrict__ x,
                                                    const __hip_bfloat16* __restrict__ wqkv,
                                                    const __hip_bfloat16* __restrict__ w1,
                                                    const __hip_bfloat16* __restrict__ w3, char* ws,
                                                    const __hip_bfloat16* __restrict__ wo,
                                                    const __hip_bfloat16* __restrict__ w2) {
  __shared__ __attribute__((aligned(16))) char smem[163840];
  const int b = blockIdx.x;
  const int xc = b & 7, ix = b >> 3;
  const int role = (xc & 1) * 8 + (ix & 7), mtile = 4 * (xc >> 1) + (ix >> 3);
  if (b == 0 && threadIdx.x == 0) *(unsigned*)(ws + WS_FLAG) = 0u;
  if (role < 8) k1_body<0>(smem, x, wqkv, w1, w3, ws, mtile, role, wo, w2);
  else k1_body<1>(smem, x, wqkv, w1, w3, ws, mtile, role, wo, w2);
}

constexpr int K2_SLOT = 32768;
constexpr int K2_NSLOT = 4;
constexpr int K2_KV = K2_NSLOT * K2_SLOT;
constexpr int K2_NGEMM = 224;

constexpr int K2_PW = 10, K2_TF = 2;

__device__ __forceinline__ bool k2_flag_ready(unsigned lds0) {
  unsigned f;
  asm volatile("ds_read_b32 %0, %1\n\ts_waitcnt lgkmcnt(0)" : "=&v"(f) : "v"(lds0 + K2_KV + 31 * 1024) : "memory");
  return __builtin_amdgcn_readfirstlane(f) >= 16u;
}

__device__ __forceinline__ void k2_loader(char* smem, const __hip_bfloat16* __restrict__ wo,
                                          const __hip_bfloat16* __restrict__ w2, char* ws, int j, int mt, int n0) {
  const int l = threadIdx.x & 63;
  const int chs = (l & 7) ^ (((l >> 4) & 3) << 1);
  const char* src_w2 = (const char*)(w2 + (size_t)(32 * j + (l >> 3)) * DM + n0 + 8 * chs);
  const char* src_wo = (const char*)(wo + (size_t)(l >> 3) * DM + n0 + 8 * chs);
  const char* src_h = ws + WS_H + ((size_t)((mt * 4 + j) * 56) * 64 + l) * 16;
  const unsigned lds0 = (unsigned)(uintptr_t)(lds_void_ptr)smem;
  auto issue = [&](auto tc) {
    constexpr int t = tc.value;
    char* slot = smem + (t % K2_NSLOT) * K2_SLOT;
    if constexpr (t == K2_PW || t == K2_PW + 1) {
#pragma unroll
      for (int i = 0; i < 8; ++i) {
        int row0 = 224 * (t - K2_PW) + 8 * (8 * j + i);
        int row = row0 + (l >> 3);
        const char* sp = src_wo + (size_t)row0 * DM * 2;
        if (row > 447) sp -= (size_t)(row - 447) * DM * 2;
        glds16(sp, slot + (8 * j + i) * 1024);
      }
    } else if constexpr (t < 16) {
      constexpr int f = t < K2_PW ? t : t - 2;
#pragma unroll
      for (int i = 0; i < 4; ++i) glds16(src_w2 + (size_t)(f * 128 + 8 * i) * DM * 2, slot + (4 * j + i) * 1024);
#pragma unroll
      for (int i = 0; i < 4; ++i) glds16(src_h + (size_t)(f * 4 + i) * 1024, slot + 16384 + (j * 4 + i) * 1024);
    }
  };
  auto issue_kv = [&]() {
#pragma unroll
    for (int i = 0; i < 8; ++i)
      __builtin_amdgcn_global_load_lds((gbl_cvoid_ptr)(ws + WS_KVR + (size_t)((8 * j + i) * 64 + l) * 16),
                                       (lds_void_ptr)(smem + K2_KV + (8 * j + i) * 1024), 16, 0, 16);
  };
  static_for<0, K2_NSLOT - 1>([&](auto tc) { issue(tc); });
  bool kv_ready = false;
  static_for<0, 16>([&](auto tc) {
    constexpr int t = tc.value;
    wait_vm<8 * ((K2_NSLOT - 2) < (15 - t) ? (K2_NSLOT - 2) : (15 - t))>();
    bar();
    if constexpr (t == K2_PW) {
      if (!kv_ready) {
        unsigned spins = 0;
        while (__hip_atomic_load((unsigned*)(ws + WS_FLAG), __ATOMIC_RELAXED, __HIP_MEMORY_SCOPE_AGENT) < 16u) {
          __builtin_amdgcn_s_sleep(2);
          if (++spins > (1u << 24)) break;
        }
        asm volatile("" ::: "memory");
        issue_kv();
        wait_vm<0>();
        bar();
      }
    }
    issue(ic<t + K2_NSLOT - 1>{});
    if constexpr (t == K2_TF) {
      if (j == 0)
        __builtin_amdgcn_global_load_lds((gbl_cvoid_ptr)(ws + WS_FLAG), (lds_void_ptr)(smem + K2_KV + 31 * 1024), 4, 0, 16);
    }
    if constexpr (t == K2_TF + 3) kv_ready = k2_flag_ready(lds0);
    if constexpr (t == K2_TF + 4) {
      if (kv_ready) issue_kv();
    }
  });
  bar();
}

__device__ __forceinline__ void k2_gemm(char* smem, const __hip_bfloat16* __restrict__ x, char* ws,
                                        __hip_bfloat16* __restrict__ out, int w, int mt, int n0) {
  const int tid = threadIdx.x, l = tid & 63;
  const int rt = w & 3, kg = w >> 2;
  const int g = l >> 4, q = (l & 15) >> 2, p = l & 3;

  u32x4 pqf[8];
#pragma unroll
  for (int hh = 0; hh < 8; ++hh)
    pqf[hh] = *(const u32x4*)(ws + WS_PQ + ((size_t)((mt * 4 + rt) * 16 + 2 * hh + kg) * 64 + l) * 16);
  const int orow = mt * 64 + rt * 16 + (l & 15);
  u32x2 xr[2];
#pragma unroll
  for (int i = 0; i < 2; ++i) xr[i] = *(const u32x2*)(x + (size_t)orow * DM + n0 + (2 * kg + i) * 16 + 4 * g);
  asm volatile("" ::: "memory");

  f32x4 acc1[4], acc2[4];
#pragma unroll
  for (int c = 0; c < 4; ++c) { acc1[c] = f32x4{0.f, 0.f, 0.f, 0.f}; acc2[c] = acc1[c]; }

  int offa[4], offwa[4], offwb[4];
#pragma unroll
  for (int c = 0; c < 4; ++c) {
    int r1 = 4 * g + q;
    offa[c] = r1 * 128 + ((c ^ ((r1 >> 1) & 3)) << 5) + 8 * p;
    int r2 = 16 + 4 * (g > 2 ? 2 : g) + q;
    offwa[c] = (28 * kg + r1) * 128 + ((c ^ ((2 * kg + (r1 >> 1)) & 3)) << 5) + 8 * p;
    offwb[c] = (28 * kg + r2) * 128 + ((c ^ ((2 * kg + (r2 >> 1)) & 3)) << 5) + 8 * p;
  }
  bool kv_ready = false;
  const unsigned lds0 = (unsigned)(uintptr_t)(lds_void_ptr)smem;
  bf16x8 of[8];

  static_for<0, 16>([&](auto tc) {
    constexpr int t = tc.value;
    bar();
    const char* slot = smem + (t % K2_NSLOT) * K2_SLOT;
    if constexpr (t == K2_PW || t == K2_PW + 1) {
      if constexpr (t == K2_PW) {
        if (!kv_ready) bar();
        f32x4 n0v[8], n1v[8];
#pragma unroll
        for (int hh = 0; hh < 8; ++hh) {
          bf16x8 pq = __builtin_bit_cast(bf16x8, pqf[hh]);
          const char* kp = smem + K2_KV + (2 * hh + kg) * 2048 + l * 16;
          n0v[hh] = mfma16(*(const bf16x8*)kp, pq, f32x4{0.f, 0.f, 0.f, 0.f});
          n1v[hh] = mfma16(*(const bf16x8*)(kp + 1024), pq, f32x4{0.f, 0.f, 0.f, 0.f});
        }
        float den[8];
#pragma unroll
        for (int hh = 0; hh < 8; ++hh) den[hh] = __shfl(n1v[hh][0], (l & 15) + 48);
#pragma unroll
        for (int hh = 0; hh < 8; ++hh) {
          const float inv = rcp_(den[hh] + 1e-6f);
          const float invm = g == 3 ? 0.f : inv;
          u32x4 ov = {pack2(n0v[hh][0] * inv, n0v[hh][1] * inv), pack2(n0v[hh][2] * inv, n0v[hh][3] * inv),
                      pack2(n1v[hh][0] * invm, n1v[hh][1] * invm), pack2(n1v[hh][2] * invm, n1v[hh][3] * invm)};
          of[hh] = __builtin_bit_cast(bf16x8, ov);
        }
      }
#pragma unroll
      for (int i = 0; i < 4; ++i) {
        const int hh = (t - K2_PW) * 4 + i;
#pragma unroll
        for (int c = 0; c < 4; ++c) {
          bf16x8 bfg = tr_frag(slot + i * 7168 + offwa[c], slot + i * 7168 + offwb[c]);
          acc2[c] = mfma16(bfg, of[hh], acc2[c]);
        }
      }
    } else {
      const bf16x8 hf0 = *(const bf16x8*)(slot + 16384 + (rt * 4 + kg) * 1024 + l * 16);
      const bf16x8 hf1 = *(const bf16x8*)(slot + 16384 + (rt * 4 + kg + 2) * 1024 + l * 16);
      bf16x8 bA[4], bB[4];
#pragma unroll
      for (int c = 0; c < 4; ++c) bA[c] = tr_frag(slot + kg * 4096 + offa[c], slot + kg * 4096 + 2048 + offa[c]);
#pragma unroll
      for (int c = 0; c < 4; ++c) bB[c] = tr_frag(slot + (kg + 2) * 4096 + offa[c], slot + (kg + 2) * 4096 + 2048 + offa[c]);
#pragma unroll
      for (int c = 0; c < 4; ++c) acc1[c] = mfma16(bA[c], hf0, acc1[c]);
#pragma unroll
      for (int c = 0; c < 4; ++c) acc1[c] = mfma16(bB[c], hf1, acc1[c]);
    }
    if constexpr (t == K2_TF + 3) kv_ready = k2_flag_ready(lds0);
  });


  {
    char* ex = smem + K2_SLOT + w * 4096;
    *(f32x4*)(ex + 0 * 1024 + l * 16) = kg ? acc1[0] : acc1[2];
    *(f32x4*)(ex + 1 * 1024 + l * 16) = kg ? acc1[1] : acc1[3];
    *(f32x4*)(ex + 2 * 1024 + l * 16) = kg ? acc2[0] : acc2[2];
    *(f32x4*)(ex + 3 * 1024 + l * 16) = kg ? acc2[1] : acc2[3];
  }
  bar();
  {
    const char* ex = smem + K2_SLOT + (w ^ 4) * 4096;
#pragma unroll
    for (int i = 0; i < 2; ++i) {
      f32x4 a1 = (kg ? acc1[2 + i] : acc1[i]) + *(const f32x4*)(ex + i * 1024 + l * 16);
      f32x4 a2 = (kg ? acc2[2 + i] : acc2[i]) + *(const f32x4*)(ex + (2 + i) * 1024 + l * 16);
      const int col = n0 + (2 * kg + i) * 16 + 4 * g;
      float xv[4] = {bf_lo(xr[i][0]), bf_hi(xr[i][0]), bf_lo(xr[i][1]), bf_hi(xr[i][1])};
      float r[4];
#pragma unroll
      for (int jj = 0; jj < 4; ++jj) r[jj] = xv[jj] + bfr(a2[jj]) * bfr(silu_(bfr(a1[jj])));
      u32x2 ov = {pack2(r[0], r[1]), pack2(r[2], r[3])};
      *(u32x2*)(out + (size_t)orow * DM + col) = ov;
    }
  }

}

__global__ __launch_bounds__(768, 3) void k2_kernel(const __hip_bfloat16* __restrict__ x,
                                                    const __hip_bfloat16* __restrict__ wo,
                                                    const __hip_bfloat16* __restrict__ w2,
                                                    char* ws, __hip_bfloat16* __restrict__ out) {
  __shared__ __attribute__((aligned(16))) char smem[K2_NSLOT * K2_SLOT + 32768];
  const int tid = threadIdx.x, l = tid & 63;
  const int b = blockIdx.x;
  if (b >= K2_NGEMM) {
    const int head = b - K2_NGEMM;
    if (tid < 128) {
      const int et = tid >> 6;
      f32x4 s0 = f32x4{0.f, 0.f, 0.f, 0.f}, s1 = s0;
#pragma unroll
      for (int m = 0; m < 16; ++m) {
        const char* pb = ws + WS_KVP + ((size_t)(m * 16 + head) * 256) * 16;
        s0 += *(const f32x4*)(pb + ((0 * 2 + et) * 64 + l) * 16);
        s1 += *(const f32x4*)(pb + ((1 * 2 + et) * 64 + l) * 16);
      }
      if ((l >> 4) == 3) s1 = f32x4{0.f, 0.f, 0.f, 0.f};
      if (et == 1 && (l & 15) > 12) { s0 = f32x4{0.f, 0.f, 0.f, 0.f}; s1 = s0; }
      u32x4 o = {pack2(s0[0], s0[1]), pack2(s0[2], s0[3]), pack2(s1[0], s1[1]), pack2(s1[2], s1[3])};
      const __amdgpu_buffer_rsrc_t rs = __builtin_amdgcn_make_buffer_rsrc(ws + WS_KVR, 0, 32768, 0x00020000);
      __builtin_amdgcn_raw_buffer_store_b128(o, rs, (unsigned)(((head * 2 + et) * 64 + l) * 16), 0, 16);
      asm volatile("s_waitcnt vmcnt(0)" ::: "memory");
    }
    __syncthreads();
    if (tid == 0)
      __hip_atomic_fetch_add((unsigned*)(ws + WS_FLAG), 1u, __ATOMIC_RELAXED, __HIP_MEMORY_SCOPE_AGENT);
    return;
  }
  const int xcd = b & 7, idx = b >> 3;
  const int mt = 4 * xcd + idx / 7, n0 = (idx % 7) * 64;
  const int w = __builtin_amdgcn_readfirstlane(tid >> 6);
  if (w >= 8) k2_loader(smem, wo, w2, ws, w - 8, mt, n0);
  else k2_gemm(smem, x, ws, out, w, mt, n0);
}

extern "C" void kernel_launch(void* const* d_in, const int* in_sizes, int n_in, void* d_out, int out_size,
                              void* d_ws, size_t ws_size, hipStream_t stream) {
  const __hip_bfloat16* x = (const __hip_bfloat16*)d_in[0];
  const __hip_bfloat16* wqkv = (const __hip_bfloat16*)d_in[1];
  const __hip_bfloat16* wo = (const __hip_bfloat16*)d_in[2];
  const __hip_bfloat16* w1 = (const __hip_bfloat16*)d_in[4];
  const __hip_bfloat16* w3 = (const __hip_bfloat16*)d_in[5];
  const __hip_bfloat16* w2 = (const __hip_bfloat16*)d_in[6];
  char* ws = (char*)d_ws;
  k1_kernel<<<256, 512, 0, stream>>>(x, wqkv, w1, w3, ws, wo, w2);
  k2_kernel<<<K2_NGEMM + 16, 768, 0, stream>>>(x, wo, w2, ws, (__hip_bfloat16*)d_out);
}
```
